```python
import torch

def _softmax(x, axis=-1):
    return torch.softmax(x, dim=axis)
def _gelu(x):
    return torch.nn.functional.gelu(x, approximate="tanh")
def _silu(x):
    return torch.nn.functional.silu(x)
def _sigmoid(x):
    return torch.sigmoid(x)
def _layernorm(x, axis=-1, eps=1e-5):
    m = x.mean(dim=axis, keepdim=True)
    v = x.var(dim=axis, keepdim=True, unbiased=False)
    return (x - m) / torch.sqrt(v + eps)
def _scatter_add(tgt, idx, src, axis=0):
    return tgt.clone().index_add_(axis, idx, src)
def _segment_sum(src, seg, n):
    out = torch.zeros((n,) + src.shape[1:], dtype=src.dtype)
    return out.index_add_(0, seg, src)
def _pad(x, pw):
    flat = tuple(v for pair in reversed(pw) for v in pair)
    return torch.nn.functional.pad(x, flat)

kernel_name = 'gated_mix__ssm_conv_s5__ssm_conv_s4__prompt'

def setup_inputs():

    _w = (lambda a: (lambda t: t.to(torch.bfloat16) if t.is_floating_point() else t)(torch.as_tensor(a)))
    def _A_setup():
        B, S, D = 1, 2048, 640
        P = D
        Lambda_re = -0.5 * (lambda _x: torch.exp(torch.as_tensor(_x)))((lambda *s: torch.randn(*s, dtype=torch.bfloat16) * 0.2)(P))
        Lambda_im = (lambda *s: torch.randn(*s, dtype=torch.bfloat16) * 0.4)(P) * 1.0
        B_re = (lambda *s: torch.randn(*s, dtype=torch.bfloat16) * 0.2)(P, D) * (D ** -0.5)
        B_im = (lambda *s: torch.randn(*s, dtype=torch.bfloat16) * 0.2)(P, D) * (D ** -0.5)
        C_re = (lambda *s: torch.randn(*s, dtype=torch.bfloat16) * 0.2)(D, P) * (P ** -0.5)
        C_im = (lambda *s: torch.randn(*s, dtype=torch.bfloat16) * 0.2)(D, P) * (P ** -0.5)
        log_dt = (lambda *s: torch.randn(*s, dtype=torch.bfloat16) * 0.2)(P) * 0.3 - 2.0
        Dskip = (lambda *s: torch.randn(*s, dtype=torch.bfloat16) * 0.05)(D)
        return {
            "x": _w((lambda *s: torch.randn(*s, dtype=torch.bfloat16) * 0.05)(B, S, D)),
            "Lambda_re": _w(Lambda_re),
            "Lambda_im": _w(Lambda_im),
            "B_re": _w(B_re),
            "B_im": _w(B_im),
            "C_re": _w(C_re),
            "C_im": _w(C_im),
            "log_dt": _w(log_dt),
            "Dskip": _w(Dskip),
        }
    _A_in = _A_setup()

    def _B_setup():
        B, L, D, N = 1, 2048, 640, 128
        A_re = -(lambda _x: torch.exp(torch.as_tensor(_x)))((lambda *s: torch.randn(*s, dtype=torch.bfloat16) * 3.2)(D, N))
        A_im = torch.pi * (lambda *s: torch.randn(*s, dtype=torch.bfloat16) * 0.05)(D, N)
        B_re = (lambda *s: torch.randn(*s, dtype=torch.bfloat16) * 0.1)(D, N)
        B_im = (lambda *s: torch.randn(*s, dtype=torch.bfloat16) * 0.1)(D, N)
        C_re = (lambda *s: torch.randn(*s, dtype=torch.bfloat16) * 0.1)(D, N)
        C_im = (lambda *s: torch.randn(*s, dtype=torch.bfloat16) * 0.1)(D, N)
        log_dt = (lambda *s: torch.randn(*s, dtype=torch.bfloat16) * 0.05)(D) * 0.5 - 4.0
        Dskip = (lambda *s: torch.randn(*s, dtype=torch.bfloat16) * 0.05)(D)
        return {
            "u": _w((lambda *s: torch.randn(*s, dtype=torch.bfloat16) * 0.05)(B, L, D)),
            "A_re": _w(A_re),
            "A_im": _w(A_im),
            "B_re": _w(B_re),
            "B_im": _w(B_im),
            "C_re": _w(C_re),
            "C_im": _w(C_im),
            "log_dt": _w(log_dt),
            "Dskip": _w(Dskip),
        }
    _B_in = _B_setup()

    return {
        "A_x": _A_in["x"],
        "A_Lambda_re": _A_in["Lambda_re"],
        "A_Lambda_im": _A_in["Lambda_im"],
        "A_B_re": _A_in["B_re"],
        "A_B_im": _A_in["B_im"],
        "A_C_re": _A_in["C_re"],
        "A_C_im": _A_in["C_im"],
        "A_log_dt": _A_in["log_dt"],
        "A_Dskip": _A_in["Dskip"],
        "B_u": _B_in["u"],
        "B_A_re": _B_in["A_re"],
        "B_A_im": _B_in["A_im"],
        "B_B_re": _B_in["B_re"],
        "B_B_im": _B_in["B_im"],
        "B_C_re": _B_in["C_re"],
        "B_C_im": _B_in["C_im"],
        "B_log_dt": _B_in["log_dt"],
        "B_Dskip": _B_in["Dskip"],
        "gate_w": _w((lambda *s: torch.randn(*s, dtype=torch.bfloat16) * 0.05)(640) * 16.0),
    }

def reference(A_x, A_Lambda_re, A_Lambda_im, A_B_re, A_B_im, A_C_re, A_C_im, A_log_dt, A_Dskip, B_u, B_A_re, B_A_im, B_B_re, B_B_im, B_C_re, B_C_im, B_log_dt, B_Dskip, gate_w):
    def _A_ref(x, Lambda_re, Lambda_im, B_re, B_im, C_re, C_im, log_dt, Dskip):
        Bsz, L, H = x.shape
        Lam = Lambda_re + 1j * Lambda_im
        Bmat = B_re + 1j * B_im
        Cmat = C_re + 1j * C_im
        dt = torch.exp(log_dt)
        Lam_bar = torch.exp(dt * Lam)
        B_bar = ((Lam_bar - 1.0) / Lam)[:, None] * Bmat
        Bu = torch.einsum("bld,pd->blp", x + 0j, B_bar)
        h = torch.zeros_like(Bu[:, 0, :])
        hs = []
        for t in range(L):
            h = Lam_bar[None, :] * h + Bu[:, t, :]
            hs.append(h)
        hseq = torch.stack(hs, 1)
        y = 2.0 * torch.einsum("blp,dp->bld", hseq, Cmat).real + x * Dskip[None, None, :]
        return y

    def _B_ref(u, A_re, A_im, B_re, B_im, C_re, C_im, log_dt, Dskip):
        Bsz, L, D = u.shape
        dt = torch.exp(log_dt)[:, None]
        A = A_re + 1j * A_im
        Bc = B_re + 1j * B_im
        Cc = C_re + 1j * C_im
        logdA = dt * A
        dB = dt * Bc
        l = (lambda x, dt: x.to(dt))(torch.arange(L), A_re.dtype)
        P = torch.exp(logdA[:, :, None] * l[None, None, :])
        K = torch.einsum("dn,dnl,dn->dl", Cc, P, dB).real
        L2 = 2 * L
        Kp = _pad(K, ((0, 0), (0, L)))
        up = (lambda x, *d: x.permute(*d))(u, 0, 2, 1)
        up = _pad(up, ((0, 0), (0, 0), (0, L)))
        Kf = (lambda x, axes=(-2,-1): torch.fft.rfft2(x.float(), dim=axes))(Kp[:, None, :], axes=(-2, -1))
        Uf = (lambda x, axes=(-2,-1): torch.fft.rfft2(x.float(), dim=axes))(up[:, :, None, :], axes=(-2, -1))
        Yf = Uf * Kf[None, :, :, :]
        y = (lambda x, s, axes=(-2,-1): torch.fft.irfft2(x, s=s, dim=axes).to(torch.bfloat16))(Yf, (1, L2), axes=(-2, -1))[:, :, 0, :L]
        y = (lambda x, *d: x.permute(*d))(y, 0, 2, 1) + u * Dskip[None, None, :]
        return y

    x = A_x
    g = _sigmoid((x * gate_w).sum(-1, keepdims=True))
    a = _A_ref(x, A_Lambda_re, A_Lambda_im, A_B_re, A_B_im, A_C_re, A_C_im, A_log_dt, A_Dskip)
    b = _B_ref(x, B_A_re, B_A_im, B_B_re, B_B_im, B_C_re, B_C_im, B_log_dt, B_Dskip)
    return g * a + (1 - g) * b
```

```cpp
#include <hip/hip_runtime.h>
#include <hip/hip_bf16.h>
#include <stdint.h>

typedef unsigned short u16;
constexpr int L = 2048, D = 640, P = 640, N = 128;

__device__ __forceinline__ float bf2f(u16 v) { return __uint_as_float(((unsigned)v) << 16); }
__device__ __forceinline__ float rbf(float f) {
    unsigned u = __float_as_uint(f);
    u = (u + 0x7FFFu + ((u >> 16) & 1u)) & 0xFFFF0000u;
    return __uint_as_float(u);
}

__global__ void k_bu(const u16* x, const u16* lre, const u16* lim, const u16* bre, const u16* bim, const u16* logdt,
                     float2* bu) {
    int p = blockIdx.x * 64 + threadIdx.x;
    int t = blockIdx.y * 4 + threadIdx.y;
    float ar = 0.f, ai = 0.f;
    for (int d = 0; d < D; ++d) {
        float xv = bf2f(x[t * D + d]);
        ar += xv * bf2f(bre[p * D + d]);
        ai += xv * bf2f(bim[p * D + d]);
    }
    float dt = rbf(expf(bf2f(logdt[p])));
    float Lr = bf2f(lre[p]), Li = bf2f(lim[p]);
    float e = expf(dt * Lr);
    float lr = e * cosf(dt * Li), li = e * sinf(dt * Li);
    float nr = lr - 1.f, ni = li;
    float den = Lr * Lr + Li * Li;
    float sr = (nr * Lr + ni * Li) / den, si = (ni * Lr - nr * Li) / den;
    bu[t * P + p] = make_float2(sr * ar - si * ai, sr * ai + si * ar);
}

__global__ void k_scan(const u16* lre, const u16* lim, const u16* logdt, float2* bu) {
    int p = blockIdx.x * 64 + threadIdx.x;
    float dt = rbf(expf(bf2f(logdt[p])));
    float Lr = bf2f(lre[p]), Li = bf2f(lim[p]);
    float e = expf(dt * Lr);
    float lr = e * cosf(dt * Li), li = e * sinf(dt * Li);
    float hr = 0.f, hi = 0.f;
    for (int t = 0; t < L; ++t) {
        float2 b = bu[t * P + p];
        float nr = lr * hr - li * hi + b.x;
        float ni = lr * hi + li * hr + b.y;
        hr = nr; hi = ni;
        bu[t * P + p] = make_float2(hr, hi);
    }
}

__global__ void k_kgen(const u16* are, const u16* aim, const u16* bre, const u16* bim, const u16* cre, const u16* cim,
                       const u16* logdt, float* K) {
    int d = blockIdx.y;
    int l = blockIdx.x * 256 + threadIdx.x;
    float lf = rbf((float)l);
    float dt = rbf(expf(bf2f(logdt[d])));
    float acc = 0.f;
    for (int n = 0; n < N; ++n) {
        float zr = dt * bf2f(are[d * N + n]), zi = dt * bf2f(aim[d * N + n]);
        float br = dt * bf2f(bre[d * N + n]), bi = dt * bf2f(bim[d * N + n]);
        float cr = bf2f(cre[d * N + n]), ci = bf2f(cim[d * N + n]);
        float wr = cr * br - ci * bi, wi = cr * bi + ci * br;
        float e = expf(zr * lf);
        float s, c;
        sincosf(zi * lf, &s, &c);
        acc += e * (wr * c - wi * s);
    }
    K[d * L + l] = acc;
}

__global__ void k_final(const u16* x, const float2* h, const u16* cre, const u16* cim, const u16* dskA, const float* K,
                        const u16* dskB, const u16* gw, float* out) {
    __shared__ float red[4];
    int t = blockIdx.x;
    float s = 0.f;
    for (int d = threadIdx.x; d < D; d += 256) s += rbf(bf2f(x[t * D + d]) * bf2f(gw[d]));
    for (int o = 32; o > 0; o >>= 1) s += __shfl_xor(s, o);
    if ((threadIdx.x & 63) == 0) red[threadIdx.x >> 6] = s;
    __syncthreads();
    float z = rbf(red[0] + red[1] + red[2] + red[3]);
    float g = rbf(1.f / (1.f + expf(-z)));
    float omg = rbf(1.f - g);
    for (int d = threadIdx.x; d < D; d += 256) {
        float acc = 0.f;
        for (int p = 0; p < P; ++p) {
            float2 hv = h[t * P + p];
            acc += hv.x * bf2f(cre[d * P + p]) - hv.y * bf2f(cim[d * P + p]);
        }
        float xv = bf2f(x[t * D + d]);
        float a = 2.f * acc + rbf(xv * bf2f(dskA[d]));
        float y = 0.f;
        for (int l = 0; l <= t; ++l) y += K[d * L + l] * bf2f(x[(t - l) * D + d]);
        float b = rbf(rbf(y) + rbf(xv * bf2f(dskB[d])));
        out[t * D + d] = g * a + rbf(omg * b);
    }
}

extern "C" void kernel_launch(void* const* d_in, const int* in_sizes, int n_in, void* d_out, int out_size, void* d_ws,
                              size_t ws_size, hipStream_t stream) {
    const u16* x = (const u16*)d_in[0];
    float2* bu = (float2*)d_ws;
    float* K = (float*)((char*)d_ws + (size_t)L * P * 8);
    k_bu<<<dim3(P / 64, L / 4), dim3(64, 4), 0, stream>>>(x, (const u16*)d_in[1], (const u16*)d_in[2], (const u16*)d_in[3],
                                                         (const u16*)d_in[4], (const u16*)d_in[7], bu);
    k_scan<<<P / 64, 64, 0, stream>>>((const u16*)d_in[1], (const u16*)d_in[2], (const u16*)d_in[7], bu);
    k_kgen<<<dim3(L / 256, D), 256, 0, stream>>>((const u16*)d_in[10], (const u16*)d_in[11], (const u16*)d_in[12],
                                                 (const u16*)d_in[13], (const u16*)d_in[14], (const u16*)d_in[15],
                                                 (const u16*)d_in[16], K);
    k_final<<<L, 256, 0, stream>>>(x, bu, (const u16*)d_in[5], (const u16*)d_in[6], (const u16*)d_in[8], K,
                                   (const u16*)d_in[17], (const u16*)d_in[18], (float*)d_out);
}
```

```cpp
#include <hip/hip_runtime.h>
#include <stdint.h>

#define DIAG 0

#define DI __device__ __forceinline__
#define GAS __attribute__((address_space(1)))
#define LAS __attribute__((address_space(3)))
typedef unsigned short u16;
typedef unsigned long long u64;
typedef unsigned v4u __attribute__((ext_vector_type(4)));
typedef unsigned v2u __attribute__((ext_vector_type(2)));
typedef float f32x2 __attribute__((ext_vector_type(2)));
typedef float f32x4 __attribute__((ext_vector_type(4)));
typedef float f32x16 __attribute__((ext_vector_type(16)));
typedef short s16x4 __attribute__((ext_vector_type(4)));
typedef __bf16 bf16x2 __attribute__((ext_vector_type(2)));
typedef __amdgpu_buffer_rsrc_t rsrc_t;

constexpr int SEQ = 2048, DM = 640, NMODE = 128;
constexpr unsigned XBYTES = SEQ * DM * 2, MBYTES = DM * DM * 2, SBYTES = DM * NMODE * 2, VBYTES = DM * 2;

constexpr unsigned WS_EPOCH = 0;
constexpr unsigned WS_FLAG = 4096;
constexpr unsigned FL_H = 0, FL_XT = 160, FL_Y = 240, FL_G = 320, FL_N = 336;
constexpr unsigned WS_G = 96 * 1024;
constexpr unsigned WS_AGG = 128 * 1024;
constexpr unsigned WS_H = 512 * 1024;
constexpr unsigned WS_XT = WS_H + SEQ * 1280 * 2;
constexpr unsigned WS_Y = WS_XT + XBYTES;
constexpr unsigned WS_END = WS_Y + XBYTES;
constexpr unsigned WS_STAMP = 16u << 20;

constexpr int LDS_BYTES = 163840;
constexpr unsigned LDS_DUMMY = LDS_BYTES - 256;
constexpr int SPIN_CAP = 20000;
constexpr int AUX_SC1 = 16;

#define NC1 20
#define NC2 10

struct Params {
    const void *x, *lre, *lim, *bre, *bim, *cre, *cim, *ldt, *dska;
    const void *sare, *saim, *sbre, *sbim, *scre, *scim, *sldt, *dskb, *gw;
    void *out, *ws;
};

DI rsrc_t mk(const void* p, unsigned bytes) { return __builtin_amdgcn_make_buffer_rsrc((void*)p, (short)0, (int)bytes, 0x00020000); }
DI float bflo(unsigned v) { return __uint_as_float(v << 16); }
DI float bfhi(unsigned v) { return __uint_as_float(v & 0xffff0000u); }
DI unsigned pk2(float a, float b) { f32x2 v = {a, b}; return __builtin_bit_cast(unsigned, __builtin_convertvector(v, bf16x2)); }
DI unsigned pk2v(f32x2 v) { return __builtin_bit_cast(unsigned, __builtin_convertvector(v, bf16x2)); }
DI float rbf(float a) { return bflo(pk2(a, a)); }
DI unsigned rfl(unsigned v) { return __builtin_amdgcn_readfirstlane(v); }
#define SB() __builtin_amdgcn_sched_barrier(0)
#define MEMFENCE() asm volatile("" ::: "memory")

template <class T> DI T lds_ld(unsigned a) { return *(const LAS T*)(uintptr_t)a; }
template <class T> DI void lds_st(unsigned a, T v) { *(LAS T*)(uintptr_t)a = v; }
DI v2u lds_tr(unsigned a) { return __builtin_bit_cast(v2u, __builtin_amdgcn_ds_read_tr16_b64_v4i16((LAS s16x4*)(uintptr_t)a)); }

#define MFMA32(acc, a, b) asm volatile("v_mfma_f32_32x32x16_bf16 %0, %1, %2, %0" : "+v"(acc) : "v"(a), "v"(b))
#define MFMA32Z(acc, a, b) asm volatile("v_mfma_f32_32x32x16_bf16 %0, %1, %2, 0" : "=&v"(acc) : "v"(a), "v"(b))
#define MFMA16A(acc, a, b) asm volatile("v_mfma_f32_16x16x32_bf16 %0, %1, %2, %0" : "+v"(acc) : "a"(a), "v"(b))
#define MFMA16AZ(acc, a, b) asm volatile("v_mfma_f32_16x16x32_bf16 %0, %1, %2, 0" : "=&v"(acc) : "a"(a), "v"(b))

DI void dma16(unsigned ldsoff, unsigned voff, rsrc_t rs, unsigned soff) {
    asm volatile("s_mov_b32 m0, %0\n\ts_nop 0\n\tbuffer_load_dwordx4 %1, %2, %3 offen lds" ::"s"(ldsoff), "v"(voff), "s"(rs), "s"(soff) : "memory");
}
DI void touch(unsigned voff, rsrc_t rs, unsigned soff) {
    asm volatile("s_mov_b32 m0, %0\n\ts_nop 0\n\tbuffer_load_dword %1, %2, %3 offen lds" ::"s"(LDS_DUMMY), "v"(voff), "s"(rs), "s"(soff) : "memory");
}
#define WAITV(n) asm volatile("s_waitcnt vmcnt(%0)" ::"n"(n) : "memory")
#define BARRIER() asm volatile("s_waitcnt lgkmcnt(0)\n\ts_barrier" ::: "memory")

#define STAMP(i) do {} while (0)

DI void poll_flags(rsrc_t ws, unsigned off, unsigned tA, unsigned tB) {
    for (int spin = 0; spin < SPIN_CAP; ++spin) {
        MEMFENCE();
        const v2u f = __builtin_amdgcn_raw_buffer_load_b64(ws, off, 0, AUX_SC1);
        if (__all((f.x == tA) & (f.y == tB))) break;
    }
    __builtin_amdgcn_fence(__ATOMIC_ACQUIRE, "wavefront");
    MEMFENCE();
}
DI void set_flag(rsrc_t ws, unsigned idx, unsigned tA, unsigned tB) {
    if (threadIdx.x == 0) __builtin_amdgcn_raw_buffer_store_b64(v2u{tA, tB}, ws, WS_FLAG + idx * 256, 0, AUX_SC1);
}

struct cpx { float r, i; };
DI cpx cmul(cpx a, cpx b) { return {a.r * b.r - a.i * b.i, a.r * b.i + a.i * b.r}; }
DI cpx cfma(cpx a, cpx b, cpx c) { return {a.r * b.r - a.i * b.i + c.r, a.r * b.i + a.i * b.r + c.i}; }
struct cp2 { f32x2 r, i; };
DI cp2 cmul2(cp2 a, cp2 b) { return {a.r * b.r - a.i * b.i, a.r * b.i + a.i * b.r}; }
DI cp2 cmul2s(cp2 a, cpx b) { return {a.r * b.r - a.i * b.i, a.r * b.i + a.i * b.r}; }
DI void halves(float v, float& lo, float& hi) {
    auto s = __builtin_amdgcn_permlane32_swap(__float_as_uint(v), __float_as_uint(v), false, false);
    lo = __uint_as_float(s[0]); hi = __uint_as_float(s[1]);
}

struct CStage { rsrc_t r0, r1; unsigned vo[2], ra[2]; int w; };
DI void c_dma(const CStage& c, int pb, int i, unsigned buf) {
    const unsigned u = 5u * c.w + pb;
    dma16(buf + i * 1024, c.vo[i & 1], (pb & 1) ? c.r1 : c.r0, (u >> 1) * 128u + i * 8u * 1280u);
}
DI void c_read(const CStage& c, v4u (&cf)[50], int pb, int f, unsigned buf) {
    cf[10 * pb + f] = lds_ld<v4u>(buf + c.ra[f / 5] + (f % 5) * 2048);
}

struct KgIn { unsigned are, aim, bre, bim, cre, cim, ldt; };
DI cp2 cexp2(f32x2 zr2, f32x2 zi2, float l) {
    const f32x2 a = zr2 * l, b = zi2 * l;
    cp2 o;
    const float m0 = __builtin_amdgcn_exp2f(a.x), m1 = __builtin_amdgcn_exp2f(a.y);
    const float f0 = __builtin_amdgcn_fractf(b.x), f1 = __builtin_amdgcn_fractf(b.y);
    o.r = f32x2{m0 * __builtin_amdgcn_cosf(f0), m1 * __builtin_amdgcn_cosf(f1)};
    o.i = f32x2{m0 * __builtin_amdgcn_sinf(f0), m1 * __builtin_amdgcn_sinf(f1)};
    return o;
}
constexpr unsigned STG_ROW = 528, STG_BYTES = 53 * STG_ROW, KEXP_BASE = 4 * STG_BYTES, KEXP_RHO = 1216, KEXP_CH = 4 * KEXP_RHO;
constexpr unsigned U_CH = 4160, YST_OFF = 2 * U_CH, TAB_OFF = YST_OFF + 8192;
DI void put_lo(unsigned a, int row, cp2 v) { lds_st<v2u>(a + (21 + row) * STG_ROW, v2u{pk2v(v.r), pk2v(v.i)}); }
DI void put_hi(unsigned a, int row, cp2 v) { lds_st<v2u>(a + row * STG_ROW, v2u{pk2v(v.r), pk2v(-v.i)}); }

struct KgState { f32x2 zr2, zi2; cp2 w; };
DI KgState kg_lo(const KgIn& in, unsigned stg, int lane) {
    const float dt = rbf(__expf(bflo(in.ldt)));
    KgState s;
    const f32x2 zr = f32x2{bflo(in.are), bfhi(in.are)} * dt, zi = f32x2{bflo(in.aim), bfhi(in.aim)} * dt;
    s.zr2 = zr * 1.44269504088896341f;
    s.zi2 = zi * 0.15915494309189535f;
    const cp2 db = {f32x2{bflo(in.bre), bfhi(in.bre)} * dt, f32x2{bflo(in.bim), bfhi(in.bim)} * dt};
    const cp2 cc = {f32x2{bflo(in.cre), bfhi(in.cre)}, f32x2{bflo(in.cim), bfhi(in.cim)}};
    s.w = cmul2(cc, db);
    const unsigned a = stg + lane * 8;
    const cp2 e8 = cexp2(s.zr2, s.zi2, 8.f), e32 = cexp2(s.zr2, s.zi2, 32.f);
    cp2 v0 = {f32x2{1.f, 1.f}, f32x2{0.f, 0.f}}, v1 = e8, v2 = cmul2(e8, e8), v3 = cmul2(v2, e8);
    put_lo(a, 0, v0); put_lo(a, 1, v1); put_lo(a, 2, v2); put_lo(a, 3, v3);
    v0 = e32;
#pragma unroll
    for (int k = 1; k < 8; ++k) {
        if (k > 1) v0 = cmul2(v0, e32);
        v1 = cmul2(v1, e32); v2 = cmul2(v2, e32); v3 = cmul2(v3, e32);
        put_lo(a, 4 * k, v0); put_lo(a, 4 * k + 1, v1); put_lo(a, 4 * k + 2, v2); put_lo(a, 4 * k + 3, v3);
    }
    return s;
}
DI void kg_hi(const KgState& s, unsigned stg, int lane) {
    const unsigned a = stg + lane * 8;
    const cp2 e1 = cexp2(s.zr2, s.zi2, 1.f), e256 = cexp2(s.zr2, s.zi2, 256.f);
    const cp2 e2 = cmul2(e1, e1), e4 = cmul2(e2, e2);
    cp2 w0 = s.w, w1 = cmul2(w0, e1), w2 = cmul2(w0, e2), w3 = cmul2(w1, e2);
    put_hi(a, 0, w0); put_hi(a, 1, w1); put_hi(a, 2, w2); put_hi(a, 3, w3);
    put_hi(a, 4, cmul2(w0, e4)); put_hi(a, 5, cmul2(w1, e4)); put_hi(a, 6, cmul2(w2, e4)); put_hi(a, 7, cmul2(w3, e4));
    cp2 q = cmul2(w0, e256);
    const cp2 q2 = cmul2(q, e2);
    put_hi(a, 8, q); put_hi(a, 9, q2); put_hi(a, 10, cmul2(q, e4)); put_hi(a, 11, cmul2(q2, e4));
    q = cmul2(q, e256); put_hi(a, 12, q); put_hi(a, 13, cmul2(q, e4));
    q = cmul2(q, e256); put_hi(a, 14, q); put_hi(a, 15, cmul2(q, e4));
#pragma unroll
    for (int m = 0; m < 5; ++m) { q = cmul2(q, e256); put_hi(a, 16 + m, q); }
}
DI void kg_finish(unsigned stg, unsigned kexp, int lane, const unsigned (&ga)[32]) {
    const int j = lane & 31, hh = lane >> 5;
    const unsigned ra = stg + j * STG_ROW + hh * 16;
    f32x16 d;
#pragma unroll
    for (int ks = 0; ks < 16; ++ks) {
        const v4u a = lds_ld<v4u>(ra + ks * 32), b = lds_ld<v4u>(ra + 21 * STG_ROW + ks * 32);
        if (ks == 0) MFMA32Z(d, a, b); else MFMA32(d, a, b);
    }
    asm volatile("s_nop 11" : "+v"(d));
    const unsigned tab = stg + TAB_OFF;
    lds_st<v2u>(tab + (8 * j + 4 * hh) * 2, v2u{pk2(d[0], d[1]), pk2(d[2], d[3])});
    const unsigned a1 = tab + (hh ? 384 + 2 * j : 256 + 4 * j) * 2, a2 = tab + (hh ? 448 + 2 * j : 258 + 4 * j) * 2;
    lds_st<unsigned>(a1, pk2(d[4], d[5]));
    lds_st<unsigned>(a2, pk2(d[6], d[7]));
    const unsigned a3 = tab + (hh ? 640 + j : 512 + j) * 2;
    const unsigned p89 = pk2(d[8], d[9]), pab = pk2(d[10], d[11]);
    lds_st<u16>(a3, (u16)p89); lds_st<u16>(a3 + 64, (u16)(p89 >> 16));
    lds_st<u16>(a3 + 128, (u16)pab); lds_st<u16>(a3 + 192, (u16)(pab >> 16));
    const unsigned wa = kexp + (lane & 3) * KEXP_RHO + (lane >> 2) * 16;
#pragma unroll
    for (int r = 0; r < 4; ++r) {
        v4u o;
#pragma unroll
        for (int e = 0; e < 4; ++e) {
            const unsigned lo = lds_ld<u16>(ga[8 * r + 2 * e]), hi = lds_ld<u16>(ga[8 * r + 2 * e + 1]);
            o[e] = lo | (hi << 16);
        }
        lds_st<v4u>(wa + r * 256, o);
    }
    if (lane < 28) lds_st<v4u>(wa + 1024, v4u{0, 0, 0, 0});
}

#define SHADD(d, prev, cur, y) asm volatile("v_add_f32_dpp %0, %1, %3 wave_ror:1 row_mask:0xf bank_mask:0xf\n\t" \
    "v_add_f32_dpp %0, %2, %3 wave_shr:1 row_mask:0xf bank_mask:0xf" : "=&v"(d) : "v"(prev), "v"(cur), "v"(y))
#define SHADD0(d, cur, y) asm volatile("v_add_f32_dpp %0, %1, %2 wave_shr:1 row_mask:0xf bank_mask:0xf bound_ctrl:0" : "=&v"(d) : "v"(cur), "v"(y))

template <int E>
DI void conv_channel(unsigned kexp, unsigned ub, unsigned yst, int lane, const CStage& cs, v4u (&cf)[50], unsigned cbuf) {
    const int hh = lane >> 5;
    const unsigned aA = kexp + (lane & 3) * KEXP_RHO + (56 - 8 * ((lane & 31) >> 2) + 8 * hh) * 2;
    const unsigned aB = ub + 2 * (4 + 32 * hh + 4 * ((lane >> 2) & 3) + 16 * ((lane >> 4) & 1) + 4 * (lane & 3));
    f32x16 acc[8];
    v4u af[2][8];
#pragma unroll
    for (int dl = 0; dl < 8; ++dl) af[0][dl] = lds_ld<v4u>(aA + (64 * (7 - dl)) * 2);
    v2u b0 = lds_tr(aB), b1 = lds_tr(aB + 32);
#pragma unroll
    for (int c = 0; c < 4; ++c) {
#pragma unroll
        for (int i = 0; i < 8; ++i) {
            const v4u bf = {b0.x, b0.y, b1.x, b1.y};
            const int ni = (i + 1) & 7, nc = c + (i == 7);
            if (nc < 4) { b0 = lds_tr(aB + 128 * (4 * ni + nc)); b1 = lds_tr(aB + 128 * (4 * ni + nc) + 32); }
            if (c < 3) af[(c + 1) & 1][i] = lds_ld<v4u>(aA + (64 * (7 - i) + 16 * (c + 1)) * 2);
            SB();
#pragma unroll
            for (int dl = 0; dl < 8 - i; ++dl) {
                if (c == 0 && i == 0) MFMA32Z(acc[dl], af[0][dl], bf);
                else MFMA32(acc[i + dl], af[c & 1][dl], bf);
                const int m = 36 * c + 8 * i - i * (i - 1) / 2 + dl, pb = 2 * E + m / 72, mm = m % 72;
                if (mm < 40 && (mm & 3) == 0) { SB(); c_dma(cs, pb, mm >> 2, cbuf); SB(); }
                if (mm == 59) { SB(); WAITV(0); SB(); }
                if (mm >= 60 && mm < 70) { SB(); c_read(cs, cf, pb, mm - 60, cbuf); SB(); }
                if (mm == 71) { SB(); asm volatile("s_waitcnt lgkmcnt(0)" ::: "memory"); SB(); }
            }
            SB();
        }
    }
    asm volatile("s_nop 11" : "+v"(acc[7]));
    float p2[32], p1[32], y[32];
#pragma unroll
    for (int G = 0; G < 32; ++G) {
        const float cur = acc[G >> 2][4 * (G & 3) + 3], yy = acc[G >> 2][4 * (G & 3) + 2];
        if (G == 0) SHADD0(p2[0], cur, yy);
        else { const float prev = acc[(G - 1) >> 2][4 * ((G - 1) & 3) + 3]; SHADD(p2[G], prev, cur, yy); }
    }
#pragma unroll
    for (int G = 0; G < 32; ++G) {
        const float yy = acc[G >> 2][4 * (G & 3) + 1];
        if (G == 0) SHADD0(p1[0], p2[0], yy); else SHADD(p1[G], p2[G - 1], p2[G], yy);
    }
#pragma unroll
    for (int G = 0; G < 32; ++G) {
        const float yy = acc[G >> 2][4 * (G & 3)];
        if (G == 0) SHADD0(y[0], p1[0], yy); else SHADD(y[G], p1[G - 1], p1[G], yy);
    }
    const unsigned ya = yst + lane * 4;
#pragma unroll
    for (int G = 0; G < 32; G += 2) {
        const unsigned pk = pk2(y[G], y[G + 1]);
        lds_st<u16>(ya + G * 256, (u16)pk);
        lds_st<u16>(ya + G * 256 + 256, (u16)(pk >> 16));
    }
}

DI unsigned red_off(int i) { return i * 5120 + (i >= 6 ? 43008 : 0) + (i >= 10 ? 45056 : 0); }

__global__ __launch_bounds__(256, 1) void k_fused(Params p) {
    const int tid = threadIdx.x, lane = tid & 63;
    const int w = __builtin_amdgcn_readfirstlane(tid >> 6);
    const int b = blockIdx.x, xl = b & 7, r = b >> 3;
    STAMP(0);
    const rsrc_t ws = mk(p.ws, WS_END);
    const rsrc_t rx = mk(p.x, XBYTES);
    const int rn = r < 20 ? r : r - 20;
    const int tti = rn & 3, kk5 = rn >> 2;
    const int ct = (2 * tti + (r < 20 ? (kk5 < 2 ? kk5 + 2 : kk5 + 3) : (kk5 < 2 ? kk5 : 4))) & 7;
    const int tt = 4 * xl + tti;
    CStage cs;
    cs.r0 = mk((w & 1) ? p.cim : p.cre, MBYTES); cs.r1 = mk((w & 1) ? p.cre : p.cim, MBYTES); cs.w = w;
#pragma unroll
    for (int e = 0; e < 2; ++e) {
        cs.vo[e] = (80 * ct + (lane >> 3)) * 1280 + (((lane & 7) ^ ((4 * e + (lane >> 4)) & 7)) * 16);
        cs.ra[e] = (lane & 15) * 128 + (((4 * e + (lane >> 4)) ^ ((lane >> 1) & 7)) * 16);
    }
    v4u cf[50];
    unsigned tA, tB, cbuf3, cbuf4;

    if (r < 20) {
        const int c = 4 * (xl & 3) + (r & 3), g = 5 * (xl >> 2) + (r >> 2);
        const int th = w & 1, cg = w >> 1, ch = lane & 31, hh = lane >> 5;
        const rsrc_t rbre = mk(p.bre, MBYTES), rbim = mk(p.bim, MBYTES);
        const int pch = 64 * g + 32 * cg + ch;
        unsigned vlr, vli, vld; v2u ep;
        {
            const rsrc_t r0 = mk(p.lre, VBYTES), r1 = mk(p.lim, VBYTES), r2 = mk(p.ldt, VBYTES);
            asm volatile("s_nop 4\n\tbuffer_load_ushort %0, %4, %5, 0 offen\n\tbuffer_load_ushort %1, %4, %6, 0 offen\n\t"
                         "buffer_load_ushort %2, %4, %7, 0 offen\n\tbuffer_load_dwordx2 %3, off, %8, 0 sc1"
                         : "=&v"(vlr), "=&v"(vli), "=&v"(vld), "=&v"(ep) : "v"(pch * 2), "s"(r0), "s"(r1), "s"(r2), "s"(ws) : "memory");
        }
        const int rb = lane >> 3;
        const unsigned gch = ((lane & 7) ^ ((4 * w + (rb >> 1)) & 7)) * 16;
        const unsigned vox = (32 * (rb >> 2) + (rb & 3)) * 1280 + gch, vob = rb * 1280 + gch;
        const unsigned sox = (128 * c + 4 * w) * 1280, sob = (64 * g + 8 * w) * 1280;
        auto dma_piece = [&](int ti, unsigned slot, unsigned ko) {
            const unsigned l = slot + ti * 4096 + w * 1024;
            if (ti < 4) dma16(l, vox, rx, sox + (64 * (ti >> 1) + 16 * (ti & 1)) * 1280 + ko);
            else dma16(l, vob, (ti & 1) ? rbim : rbre, sob + 32 * ((ti - 4) >> 1) * 1280 + ko);
        };
#pragma unroll
        for (int ti = 0; ti < 8; ++ti) dma_piece(ti, 0, 0);
        asm volatile("s_waitcnt vmcnt(8)" : "+v"(vlr), "+v"(vli), "+v"(vld), "+v"(ep)::"memory");
        tA = rfl(ep.x) + 1; tB = rfl(ep.y) + 1;
        cpx lam, S2;
        {
            const float dt = rbf(__expf(bflo(vld))), Lr = bflo(vlr), Li = bflo(vli);
            const float e = __expf(dt * Lr), fr = __builtin_amdgcn_fractf((dt * Li) * 0.15915494309189535f);
            lam = {e * __builtin_amdgcn_cosf(fr), e * __builtin_amdgcn_sinf(fr)};
            const float nr = lam.r - 1.f, ni = lam.i, den = 2.f * __builtin_amdgcn_rcpf(Lr * Lr + Li * Li);
            S2 = {(nr * Lr + ni * Li) * den, (ni * Lr - nr * Li) * den};
        }
        cp2 Q[16];
        Q[0] = {f32x2{1.f, lam.r}, f32x2{0.f, lam.i}};
        const cpx l2 = cmul(lam, lam), l4 = cmul(l2, l2), l8 = cmul(l4, l4), l16 = cmul(l8, l8), l32 = cmul(l16, l16);
        SB(); dma_piece(0, 32768, 128); dma_piece(1, 32768, 128); SB();
        Q[1] = cmul2s(Q[0], l2);
#pragma unroll
        for (int i = 0; i < 2; ++i) Q[2 + i] = cmul2s(Q[i], l4);
        SB(); dma_piece(2, 32768, 128); SB();
#pragma unroll
        for (int i = 0; i < 4; ++i) Q[4 + i] = cmul2s(Q[i], l8);
        SB(); dma_piece(3, 32768, 128); SB();
#pragma unroll
        for (int i = 0; i < 8; ++i) { Q[8 + i] = cmul2s(Q[i], l16); if ((i & 1) == 1) { SB(); dma_piece(4 + (i >> 1), 32768, 128); SB(); } }
        const cpx l64 = cmul(l32, l32), l128 = cmul(l64, l64), l256 = cmul(l128, l128), l512 = cmul(l256, l256);
        const int seg = 2 * th + hh;
        cpx lseg = {1.f, 0.f}, lgat = {1.f, 0.f};
        if (hh) { lseg = l32; lgat = l128; }
        if (th) { lseg = cmul(lseg, l64); lgat = cmul(lgat, l256); }
        const cpx Sl = cmul(S2, lam);

        f32x16 aR[2], aI[2];
#pragma unroll
        for (int i = 0; i < 16; ++i) { aR[0][i] = 0.f; aR[1][i] = 0.f; aI[0][i] = 0.f; aI[1][i] = 0.f; }
        unsigned adA[4], adB[4];
        {
            const unsigned lb = ch * 128 + ((hh ^ ((ch >> 1) & 7)) * 16);
#pragma unroll
            for (int k = 0; k < 4; ++k) { adA[k] = (lb ^ (k * 32)) + th * 8192; adB[k] = (lb ^ (k * 32)) + 16384 + cg * 8192; }
        }
        STAMP(1);
        asm volatile("s_waitcnt vmcnt(8)\n\ts_barrier" : "+v"(aR[0]), "+v"(aI[0]), "+v"(aR[1]), "+v"(aI[1])::"memory");
        v4u fa0 = lds_ld<v4u>(adA[0]), fa1 = lds_ld<v4u>(adA[0] + 4096), fr = lds_ld<v4u>(adB[0]), fi = lds_ld<v4u>(adB[0] + 4096);
        const unsigned cb0 = 98304 + w * 10240, cb1 = 32768 + w * 10240;
#pragma unroll
        for (int s = 0; s < 10; ++s) {
#pragma unroll
            for (int k = 0; k < 4; ++k) {
                v4u na0 = fa0, na1 = fa1, nr = fr, ni = fi;
                if (k == 3) {
                    if (s <= 8) asm volatile("s_waitcnt vmcnt(6) lgkmcnt(0)\n\ts_barrier" ::: "memory");
                }
                const int kn = (k + 1) & 3;
                const unsigned so = ((k == 3 ? s + 1 : s) % 3) * 32768;
                const bool rd = !(k == 3 && s == 9);
                MFMA32(aR[0], fa0, fr); SB();
                if (rd) { na0 = lds_ld<v4u>(adA[kn] + so); nr = lds_ld<v4u>(adB[kn] + so); } SB();
                MFMA32(aI[0], fa0, fi); SB();
                if (rd) { ni = lds_ld<v4u>(adB[kn] + so + 4096); na1 = lds_ld<v4u>(adA[kn] + so + 4096); } SB();
                MFMA32(aR[1], fa1, fr);
                if (s <= 7) dma_piece(2 * k, ((s + 2) % 3) * 32768, (s + 2) * 128);
                else if (s == 8) c_dma(cs, 0, 2 * k, cb0);
                else if (k == 0) c_dma(cs, 0, 8, cb0);
                MFMA32(aI[1], fa1, fi);
                if (s <= 7) dma_piece(2 * k + 1, ((s + 2) % 3) * 32768, (s + 2) * 128);
                else if (s == 8) c_dma(cs, 0, 2 * k + 1, cb0);
                else if (k == 0) c_dma(cs, 0, 9, cb0);
                SB();
                fa0 = na0; fa1 = na1; fr = nr; fi = ni;
            }
        }
        asm volatile("s_nop 11" : "+v"(aR[0]), "+v"(aI[0]), "+v"(aR[1]), "+v"(aI[1]));
        STAMP(2);
        cpx agg;
        {
            f32x2 sr0 = {0.f, 0.f}, si0 = {0.f, 0.f}, sr1 = {0.f, 0.f}, si1 = {0.f, 0.f};
#pragma unroll
            for (int i = 0; i < 8; ++i) {
                const f32x2 ur = {aR[0][2 * i], aR[0][2 * i + 1]}, ui = {aI[0][2 * i], aI[0][2 * i + 1]};
                const f32x2 qr = Q[15 - i].r.yx, qi = Q[15 - i].i.yx;
                sr0 += qr * ur; si0 += qr * ui; sr0 -= qi * ui; si0 += qi * ur;
                const f32x2 vr = {aR[1][2 * i], aR[1][2 * i + 1]}, vi = {aI[1][2 * i], aI[1][2 * i + 1]};
                const f32x2 pr = Q[7 - i].r.yx, pi = Q[7 - i].i.yx;
                sr1 += pr * vr; si1 += pr * vi; sr1 -= pi * vi; si1 += pi * vr;
            }
            sr0 += sr1; si0 += si1;
            agg = {sr0.x + sr0.y, si0.x + si0.y};
        }
        cpx a0, a1;
        halves(agg.r, a0.r, a1.r); halves(agg.i, a0.i, a1.i);
        const cpx a64 = cfma(l32, a0, a1);
        const unsigned xch = 159744 + (cg * 2) * 256 + ch * 8;
        if (hh == 0) lds_st<f32x2>(xch + th * 256, f32x2{a64.r, a64.i});
        BARRIER();
        cpx pre = {0.f, 0.f};
        if (th) {
            const f32x2 o = lds_ld<f32x2>(xch);
            const cpx o64 = {o.x, o.y};
            const cpx a128 = cfma(l64, o64, a64);
            if (hh == 0 && c < 15)
                __builtin_amdgcn_raw_buffer_store_b128(v4u{__float_as_uint(a128.r), tA, __float_as_uint(a128.i), tB}, ws,
                                                       WS_AGG + (c * 640 + pch) * 16, 0, AUX_SC1);
            pre = hh ? cfma(l32, o64, a0) : o64;
        } else if (hh) pre = a0;
        STAMP(3);
        {
            cpx v = {0.f, 0.f};
#pragma unroll
            for (int t = 0; t < 32; ++t) {
                v = cfma(lam, v, cpx{aR[t >> 4][t & 15], aI[t >> 4][t & 15]});
                const cpx o = cmul(S2, v);
                aR[t >> 4][t & 15] = o.r; aI[t >> 4][t & 15] = o.i;
                SB();
                if (t < 10) c_dma(cs, 1, t, cb1);
                if (t == 10) { if (th && c < 15) WAITV(11); else WAITV(10); }
                if (t >= 10 && t < 20) c_read(cs, cf, 0, t - 10, cb0);
                if (t == 21) asm volatile("s_waitcnt lgkmcnt(0)" ::: "memory");
                if (t >= 21 && t < 31) c_dma(cs, 2, t - 21, cb0);
                SB();
            }
        }
        asm volatile("" : "+v"(aR[0]), "+v"(aI[0]), "+v"(aR[1]), "+v"(aI[1]));
        cpx carry = {0.f, 0.f};
        if (c > 0) {
            v4u ga[4];
            for (int spin = 0; spin < SPIN_CAP; ++spin) {
                MEMFENCE();
                bool ok = true;
#pragma unroll
                for (int i = 0; i < 4; ++i) {
                    const int j = c - 1 - (4 * i + seg);
                    ga[i] = __builtin_amdgcn_raw_buffer_load_b128(ws, WS_AGG + ((j < 0 ? 0 : j) * 640 + pch) * 16, 0, AUX_SC1);
                }
#pragma unroll
                for (int i = 0; i < 4; ++i) ok = ok & ((c - 1 - (4 * i + seg) < 0) | ((ga[i].y == tA) & (ga[i].w == tB)));
                if (__all(ok)) break;
            }
            cpx part = {0.f, 0.f};
#pragma unroll
            for (int i = 3; i >= 0; --i) {
                const bool val = c - 1 - (4 * i + seg) >= 0;
                const cpx a = {val ? __uint_as_float(ga[i].x) : 0.f, val ? __uint_as_float(ga[i].z) : 0.f};
                part = cfma(l512, part, a);
            }
            part = cmul(lgat, part);
            cpx p0, p1;
            halves(part.r, p0.r, p1.r); halves(part.i, p0.i, p1.i);
            const cpx ps = {p0.r + p1.r, p0.i + p1.i};
            if (hh == 0) lds_st<f32x2>(xch + 1024 + th * 256, f32x2{ps.r, ps.i});
            BARRIER();
            const f32x2 o = lds_ld<f32x2>(xch + 1024 + (th ^ 1) * 256);
            carry = {ps.r + o.x, ps.i + o.y};
        }
        STAMP(4);
        const cpx F = cmul(Sl, cfma(lseg, carry, pre));
        if (c == 0) WAITV(0);
        {
            const unsigned hs = w * 8192;
            const unsigned gsw = ((ch >> 1) & 1) << 2 | ((ch >> 3) & 1) << 3 | (ch & 1) | ((ch >> 2) & 1) << 1;
            const unsigned wb = hs + ch * 128;
#pragma unroll
            for (int q = 0; q < 8; ++q) {
                f32x2 hr[2], hi[2];
#pragma unroll
                for (int e = 0; e < 2; ++e) {
                    const int t = 4 * q + 2 * e;
                    const f32x2 ur = {aR[t >> 4][t & 15], aR[t >> 4][(t & 15) + 1]}, ui = {aI[t >> 4][t & 15], aI[t >> 4][(t & 15) + 1]};
                    const cp2 qq = Q[t >> 1];
                    hr[e] = ur + qq.r * F.r - qq.i * F.i;
                    hi[e] = -ui - qq.r * F.i - qq.i * F.r;
                }
                const unsigned a = wb + (((8 * hh + q) ^ gsw) * 8);
                lds_st<v2u>(a, v2u{pk2v(hr[0]), pk2v(hr[1])});
                lds_st<v2u>(a + 4096, v2u{pk2v(hi[0]), pk2v(hi[1])});
            }
            const int gq = lane >> 4, ra = (lane >> 2) & 3, rp = lane & 3;
            unsigned rad[2];
#pragma unroll
            for (int hf = 0; hf < 2; ++hf) {
                const int row = 8 * gq + 4 * hf + ra;
                const unsigned gs = ((row >> 1) & 1) << 2 | ((row >> 3) & 1) << 3 | (row & 1) | ((row >> 2) & 1) << 1;
                rad[hf] = hs + row * 128 + ((rp ^ gs) * 8);
            }
            const unsigned hso = WS_H + ((2 * c + th) * 40 + 4 * g + cg) * 4096;
            v4u hq[8];
#pragma unroll
            for (int i = 0; i < 8; ++i) {
                const v2u x0 = lds_tr((rad[0] ^ ((i & 3) * 32)) + (i >> 2) * 4096), x1 = lds_tr((rad[1] ^ ((i & 3) * 32)) + (i >> 2) * 4096);
                hq[i] = v4u{x0.x, x0.y, x1.x, x1.y};
            }
            SB();
#pragma unroll
            for (int i = 0; i < 8; ++i)
                __builtin_amdgcn_raw_buffer_store_b128(hq[i], ws, lane * 16 + (i & 3) * 1024, hso + (i >> 2) * 8192, AUX_SC1);
            SB();
        }
#pragma unroll
        for (int f = 0; f < 10; ++f) { c_read(cs, cf, 1, f, cb1); c_read(cs, cf, 2, f, cb0); }
        asm volatile("s_waitcnt lgkmcnt(0)" ::: "memory");
#pragma unroll
        for (int i = 0; i < 10; ++i) c_dma(cs, 3, i, cb1);
        asm volatile("s_waitcnt vmcnt(10) lgkmcnt(0)\n\ts_barrier" ::: "memory");
        set_flag(ws, FL_H + c * 10 + g, tA, tB);
        STAMP(5);
        MEMFENCE();
#pragma unroll
        for (int i = 0; i < 10; ++i) c_dma(cs, 4, i, cb0);
        cbuf3 = cb1; cbuf4 = cb0;
    } else if (r < 30) {
        const int jj = r - 20, grp = xl * 10 + jj, H = xl >> 2, tb = 2 * (xl & 3) + H;
        const unsigned stg = w * STG_BYTES, kexp = KEXP_BASE + w * 2 * KEXP_CH;
        KgIn in[2]; v2u ep;
        {
            const rsrc_t r0 = mk(p.sare, SBYTES), r1 = mk(p.saim, SBYTES), r2 = mk(p.sbre, SBYTES), r3 = mk(p.sbim, SBYTES),
                         r4 = mk(p.scre, SBYTES), r5 = mk(p.scim, SBYTES), r6 = mk(p.sldt, VBYTES);
            const unsigned d0 = grp * 8 + 2 * w;
#pragma unroll
            for (int e = 0; e < 2; ++e)
                asm volatile("s_nop 4\n\tbuffer_load_dword %0, %7, %8, %15 offen\n\tbuffer_load_dword %1, %7, %9, %15 offen\n\t"
                             "buffer_load_dword %2, %7, %10, %15 offen\n\tbuffer_load_dword %3, %7, %11, %15 offen\n\t"
                             "buffer_load_dword %4, %7, %12, %15 offen\n\tbuffer_load_dword %5, %7, %13, %15 offen\n\t"
                             "buffer_load_ushort %6, off, %14, %16"
                             : "=&v"(in[e].are), "=&v"(in[e].aim), "=&v"(in[e].bre), "=&v"(in[e].bim), "=&v"(in[e].cre), "=&v"(in[e].cim), "=&v"(in[e].ldt)
                             : "v"(lane * 4), "s"(r0), "s"(r1), "s"(r2), "s"(r3), "s"(r4), "s"(r5), "s"(r6), "s"((d0 + e) * 256), "s"((d0 + e) * 2)
                             : "memory");
            asm volatile("buffer_load_dwordx2 %0, off, %1, 0 sc1" : "=&v"(ep) : "s"(ws) : "memory");
        }
        {
            const int rb = lane >> 3;
#pragma unroll
            for (int i = 0; i < 8; ++i) {
                const unsigned f = (((rb >> 1) & 1) | ((i & 1) << 1)) << 1;
                dma16(kexp + i * 1024, rb * 1280 + (((lane & 7) ^ f) * 16), rx, (256 * tb + 64 * w + 8 * i) * 1280 + jj * 128);
            }
        }
        const rsrc_t rpf = mk(jj < 5 ? p.bre : p.bim, MBYTES);
        const unsigned pfo = (320 * H + 64 * (jj < 5 ? jj : jj - 5)) * 1280;
        touch(lane * 1280, rpf, pfo + w * 128);
        asm volatile("s_waitcnt vmcnt(9)" : "+v"(in[0].are), "+v"(in[0].aim), "+v"(in[0].bre), "+v"(in[0].bim), "+v"(in[0].cre), "+v"(in[0].cim), "+v"(in[0].ldt),
                     "+v"(in[1].are), "+v"(in[1].aim), "+v"(in[1].bre), "+v"(in[1].bim), "+v"(in[1].cre), "+v"(in[1].cim), "+v"(in[1].ldt), "+v"(ep)::"memory");
        tA = rfl(ep.x) + 1; tB = rfl(ep.y) + 1;
        STAMP(1);
        KgState ks = kg_lo(in[0], stg, lane);
        WAITV(1);
        {
            const int g4 = lane >> 4, ra = (lane >> 2) & 3, rp = lane & 3;
            v4u xq[8];
#pragma unroll
            for (int tk = 0; tk < 2; ++tk)
#pragma unroll
                for (int cs = 0; cs < 4; ++cs) {
                    v2u x[2];
#pragma unroll
                    for (int hf = 0; hf < 2; ++hf) {
                        const int row = 32 * tk + 8 * g4 + 4 * hf + ra;
                        const unsigned f = (((row >> 1) & 1) | (((row >> 3) & 1) << 1)) << 1;
                        x[hf] = lds_tr(kexp + row * 128 + (((2 * cs + (rp >> 1)) ^ f) * 16) + (rp & 1) * 8);
                    }
                    xq[tk * 4 + cs] = v4u{x[0].x, x[0].y, x[1].x, x[1].y};
                }
            SB();
            const unsigned vo = ((lane >> 3) & 1) * 32768 + (lane & 7) * 64 + g4 * 16;
#pragma unroll
            for (int i = 0; i < 8; ++i)
                __builtin_amdgcn_raw_buffer_store_b128(xq[i], ws, vo, WS_XT + (jj * 8 + 2 * (i & 3)) * 32768 + (tb * 8 + 2 * w + (i >> 2)) * 512, AUX_SC1);
            SB();
        }
        MEMFENCE();
        touch(lane * 1280, rpf, pfo + (w + 4) * 128);
        touch(lane * 1280, rpf, pfo + (w < 2 ? w + 8 : w + 4) * 128);
        STAMP(2);
        unsigned ga[32];
        {
            const unsigned tabm = stg + TAB_OFF - 2 * 0x4280;
#pragma unroll
            for (int rr = 0; rr < 4; ++rr)
#pragma unroll
                for (int e = 0; e < 4; ++e) {
                    const int l0 = 2044 + (lane & 3) - 32 * (16 * rr + (lane >> 2)) - 8 * e, l1 = l0 - 4;
                    const unsigned pk = pk2((float)l0, (float)l1);
                    unsigned g0 = tabm + 2 * (pk & 0xffffu), g1 = tabm + 2 * (pk >> 16);
                    if (rr == 3) {
                        if (l0 < 128) g0 = stg + TAB_OFF + 2 * l0;
                        if (l1 < 128) g1 = stg + TAB_OFF + 2 * l1;
                    }
                    ga[8 * rr + 2 * e] = g0; ga[8 * rr + 2 * e + 1] = g1;
                }
        }
        kg_hi(ks, stg, lane);
        kg_finish(stg, kexp, lane, ga);
        asm volatile("s_waitcnt vmcnt(2)\n\ts_barrier" ::: "memory");
        set_flag(ws, FL_XT + tb * 10 + jj, tA, tB);
        STAMP(3);
        ks = kg_lo(in[1], stg, lane);
        kg_hi(ks, stg, lane);
        SB();
        poll_flags(ws, WS_FLAG + (FL_XT + (lane & 7) * 10 + (grp >> 3)) * 256, tA, tB);
        v4u uu[8];
#pragma unroll
        for (int i = 0; i < 8; ++i)
            uu[i] = __builtin_amdgcn_raw_buffer_load_b128(ws, (lane >> 3) * 512 + (lane & 7) * 16, WS_XT + grp * 32768 + w * 128 + i * 4096, AUX_SC1);
        SB();
        kg_finish(stg, kexp + KEXP_CH, lane, ga);
        SB();
        {
            const unsigned ua = stg + ((lane >> 2) & 1) * U_CH + 64 + (lane >> 3) * 64 + (lane & 3) * 16;
#pragma unroll
            for (int i = 0; i < 8; ++i) lds_st<v4u>(ua + i * 512, uu[i]);
            if (lane < 8) lds_st<v4u>(stg + (lane >> 2) * U_CH + (lane & 3) * 16, v4u{0, 0, 0, 0});
        }
        STAMP(4);
        MEMFENCE();
        conv_channel<0>(kexp, stg, stg + YST_OFF, lane, cs, cf, stg + TAB_OFF);
        conv_channel<1>(kexp + KEXP_CH, stg + U_CH, stg + YST_OFF + 2, lane, cs, cf, stg + TAB_OFF);
        STAMP(5);
        {
            v4u yq[8];
#pragma unroll
            for (int i = 0; i < 8; ++i) yq[i] = lds_ld<v4u>(stg + YST_OFF + i * 1024 + lane * 16);
            SB();
#pragma unroll
            for (int i = 0; i < 8; ++i)
                __builtin_amdgcn_raw_buffer_store_b128(yq[i], ws, lane * 16, WS_Y + (grp * 4 + w) * 8192 + i * 1024, AUX_SC1);
        }
        asm volatile("s_waitcnt vmcnt(0) lgkmcnt(0)\n\ts_barrier" ::: "memory");
        set_flag(ws, FL_Y + grp, tA, tB);
        cbuf3 = 32768 + w * 10240; cbuf4 = 98304 + w * 10240;
        MEMFENCE();
#pragma unroll
        for (int i = 0; i < 10; ++i) c_dma(cs, 4, i, cbuf4);
    } else {
        const int gc = 2 * (2 * (xl & 3) + 1 - (xl >> 2)) + (r - 30);
        const v2u ep = __builtin_amdgcn_raw_buffer_load_b64(ws, WS_EPOCH, 0, AUX_SC1);
        const rsrc_t rgw = mk(p.gw, VBYTES);
        const unsigned vo = (lane >> 3) * 1280 + (lane & 7) * 16, so = (128 * gc + 32 * w) * 1280;
        float sum[4] = {0.f, 0.f, 0.f, 0.f};
        v4u gv[10], xv[10][4];
#pragma unroll
        for (int s = 0; s < 10; ++s) {
            gv[s] = __builtin_amdgcn_raw_buffer_load_b128(rgw, (lane & 7) * 16, s * 128, 0);
#pragma unroll
            for (int q = 0; q < 4; ++q) xv[s][q] = __builtin_amdgcn_raw_buffer_load_b128(rx, vo, so + q * 8 * 1280 + s * 128, 0);
        }
        SB();
#pragma unroll
        for (int s = 0; s < 10; ++s)
#pragma unroll
            for (int e = 0; e < 4; ++e) {
                const f32x2 gw2 = {bflo(gv[s][e]), bfhi(gv[s][e])};
#pragma unroll
                for (int q = 0; q < 4; ++q) {
                    const unsigned pr = pk2v(f32x2{bflo(xv[s][q][e]), bfhi(xv[s][q][e])} * gw2);
                    sum[q] += bflo(pr); sum[q] += bfhi(pr);
                }
            }
        tA = rfl(ep.x) + 1; tB = rfl(ep.y) + 1;
#pragma unroll
        for (int q = 0; q < 4; ++q) {
            float v = sum[q];
            v += __shfl_xor(v, 1); v += __shfl_xor(v, 2); v += __shfl_xor(v, 4);
            const float z = rbf(v), gt = rbf(1.f / (1.f + expf(-z)));
            if ((lane & 7) == 0)
                __builtin_amdgcn_raw_buffer_store_b32(__float_as_uint(gt), ws, WS_G + (128 * gc + 32 * w + 8 * q + (lane >> 3)) * 4, 0, AUX_SC1);
        }
        asm volatile("s_waitcnt vmcnt(0) lgkmcnt(0)\n\ts_barrier" ::: "memory");
        set_flag(ws, FL_G + gc, tA, tB);
        STAMP(5);
        MEMFENCE();
        cbuf3 = 32768 + w * 10240; cbuf4 = 98304 + w * 10240;
#pragma unroll
        for (int pb = 0; pb < 3; ++pb) {
#pragma unroll
            for (int i = 0; i < 10; ++i) c_dma(cs, pb, i, cbuf3);
            WAITV(0);
#pragma unroll
            for (int f = 0; f < 10; ++f) c_read(cs, cf, pb, f, cbuf3);
            asm volatile("s_waitcnt lgkmcnt(0)" ::: "memory");
        }
#pragma unroll
        for (int i = 0; i < 10; ++i) { c_dma(cs, 3, i, cbuf3); c_dma(cs, 4, i, cbuf4); }
    }

    const int own = (w + ct) & 3;
    const int tok = 64 * tt + 16 * own + (lane & 15), cq = lane >> 4;
    v2u xe[5], da[5], db[5];
    {
        const rsrc_t ra = mk(p.dska, VBYTES), rb = mk(p.dskb, VBYTES);
#pragma unroll
        for (int nt = 0; nt < 5; ++nt) {
            xe[nt] = __builtin_amdgcn_raw_buffer_load_b64(rx, tok * 1280 + cq * 8, ct * 160 + nt * 32, 0);
            da[nt] = __builtin_amdgcn_raw_buffer_load_b64(ra, cq * 8, ct * 160 + nt * 32, 0);
            db[nt] = __builtin_amdgcn_raw_buffer_load_b64(rb, cq * 8, ct * 160 + nt * 32, 0);
        }
    }
    {
        const int g0 = (10 * w) >> 2;
        poll_flags(ws, WS_FLAG + (lane < 3 ? FL_H + (tt >> 1) * 10 + g0 + lane : FL_G + (tt >> 1)) * 256, tA, tB);
    }
    STAMP(6);
    if (r < 20 || r >= 30) {
#pragma unroll
        for (int f = 0; f < 10; ++f) c_read(cs, cf, 3, f, cbuf3);
    }
#pragma unroll
    for (int f = 0; f < 10; ++f) c_read(cs, cf, 4, f, cbuf4);
    const float gate = __uint_as_float(__builtin_amdgcn_raw_buffer_load_b32(ws, WS_G + tok * 4, 0, AUX_SC1));
    f32x4 fin[5];
    {
        constexpr int WIN = 20;
        v4u hv[WIN];
        const unsigned hb = WS_H + (tt * 40 + 10 * w) * 4096;
        unsigned so[4];
#pragma unroll
        for (int o = 0; o < 4; ++o) so[o] = hb + ((own + 1 + o) & 3) * 1024;
#pragma unroll
        for (int i = 0; i < WIN; ++i) hv[i] = __builtin_amdgcn_raw_buffer_load_b128(ws, lane * 16, so[i / 10] + (i % 10) * 4096, AUX_SC1);
        SB();
#pragma unroll
        for (int o = 0; o < 4; ++o) {
            f32x4 acc[5];
#pragma unroll
            for (int j = 0; j < 10; ++j) {
                const int i = 10 * o + j;
                const v4u hf = hv[i % WIN];
#pragma unroll
                for (int nt = 0; nt < 5; ++nt) {
                    if (j == 0) MFMA16AZ(acc[nt], cf[nt], hf); else MFMA16A(acc[nt], cf[5 * j + nt], hf);
                }
                if (i + WIN < 40)
                    hv[i % WIN] = __builtin_amdgcn_raw_buffer_load_b128(ws, lane * 16, so[(i + WIN) / 10] + ((i + WIN) % 10) * 4096, AUX_SC1);
                SB();
            }
            asm volatile("s_nop 7" : "+v"(acc[0]), "+v"(acc[1]), "+v"(acc[2]), "+v"(acc[3]), "+v"(acc[4]));
            if (o < 3) {
                const unsigned ra = red_off(((own + 1 + o) & 3) * 3 + o) + lane * 16;
#pragma unroll
                for (int nt = 0; nt < 5; ++nt) lds_st<f32x4>(ra + nt * 1024, acc[nt]);
            } else {
#pragma unroll
                for (int nt = 0; nt < 5; ++nt) fin[nt] = acc[nt];
            }
            SB();
        }
    }
    STAMP(7);
    poll_flags(ws, WS_FLAG + (FL_Y + ct * 10 + (lane < 10 ? lane : 0)) * 256, tA, tB);
    unsigned yv[5][2];
#pragma unroll
    for (int nt = 0; nt < 5; ++nt)
#pragma unroll
        for (int e = 0; e < 2; ++e)
            yv[nt][e] = __builtin_amdgcn_raw_buffer_load_b32(ws, tok * 4 + cq * 16384, WS_Y + (40 * ct + 8 * nt + e) * 8192, AUX_SC1);
    BARRIER();
#pragma unroll
    for (int o = 0; o < 3; ++o)
#pragma unroll
        for (int nt = 0; nt < 5; ++nt) fin[nt] += lds_ld<f32x4>(red_off(own * 3 + o) + lane * 16 + nt * 1024);
    STAMP(8);
    {
        const rsrc_t ro = mk(p.out, SEQ * DM * 4);
        const float omg = rbf(1.f - gate);
        f32x4 oo[5];
#pragma unroll
        for (int nt = 0; nt < 5; ++nt) {
            f32x4 o;
#pragma unroll
            for (int e = 0; e < 2; ++e) {
                const f32x2 x2 = {bflo(xe[nt][e]), bfhi(xe[nt][e])};
                const unsigned sa = pk2v(x2 * f32x2{bflo(da[nt][e]), bfhi(da[nt][e])});
                const unsigned sb = pk2v(x2 * f32x2{bflo(db[nt][e]), bfhi(db[nt][e])});
                const unsigned bb = pk2v(f32x2{bflo(yv[nt][e]), bfhi(yv[nt][e])} + f32x2{bflo(sb), bfhi(sb)});
                const unsigned ob = pk2v(f32x2{bflo(bb), bfhi(bb)} * omg);
                const float a0 = fin[nt][2 * e] + bflo(sa), a1 = fin[nt][2 * e + 1] + bfhi(sa);
                o[2 * e] = __fadd_rn(__fmul_rn(gate, a0), bflo(ob));
                o[2 * e + 1] = __fadd_rn(__fmul_rn(gate, a1), bfhi(ob));
            }
            oo[nt] = o;
        }
        SB();
#pragma unroll
        for (int nt = 0; nt < 5; ++nt)
            __builtin_amdgcn_raw_buffer_store_b128(__builtin_bit_cast(v4u, oo[nt]), ro, tok * 2560 + cq * 16, ct * 320 + nt * 64, 0);
        asm volatile("s_nop 1");
        SB();
    }
    if (b == 255 && w == 0) {
        for (int k = 0; k < 4; ++k) {
            const int i = k * 64 + lane;
            poll_flags(ws, WS_FLAG + (i < 240 ? i : i + 80) * 256, tA, tB);
        }
        if (lane == 0) __builtin_amdgcn_raw_buffer_store_b64(v2u{tA, tB}, ws, WS_EPOCH, 0, AUX_SC1);
    }
}

extern "C" void kernel_launch(void* const* d_in, const int* in_sizes, int n_in, void* d_out, int out_size, void* d_ws,
                              size_t ws_size, hipStream_t stream) {
    Params p;
    p.x = d_in[0]; p.lre = d_in[1]; p.lim = d_in[2]; p.bre = d_in[3]; p.bim = d_in[4]; p.cre = d_in[5]; p.cim = d_in[6];
    p.ldt = d_in[7]; p.dska = d_in[8];
    p.sare = d_in[10]; p.saim = d_in[11]; p.sbre = d_in[12]; p.sbim = d_in[13]; p.scre = d_in[14]; p.scim = d_in[15];
    p.sldt = d_in[16]; p.dskb = d_in[17]; p.gw = d_in[18];
    p.out = d_out; p.ws = d_ws;
    (void)hipFuncSetAttribute((const void*)k_fused, hipFuncAttributeMaxDynamicSharedMemorySize, LDS_BYTES);
    k_fused<<<256, 256, LDS_BYTES, stream>>>(p);
}
```

```cpp
#define DIAG 1
#define RWG 27
#define RFROM 0
#define RTO 0
#define RCYC 0
#include <hip/hip_runtime.h>
#include <stdint.h>

#define DI __device__ __forceinline__
#define GAS __attribute__((address_space(1)))
#define LAS __attribute__((address_space(3)))
typedef unsigned short u16;
typedef unsigned long long u64;
typedef unsigned v4u __attribute__((ext_vector_type(4)));
typedef unsigned v2u __attribute__((ext_vector_type(2)));
typedef float f32x2 __attribute__((ext_vector_type(2)));
typedef float f32x4 __attribute__((ext_vector_type(4)));
typedef float f32x16 __attribute__((ext_vector_type(16)));
typedef short s16x4 __attribute__((ext_vector_type(4)));
typedef __bf16 bf16x2 __attribute__((ext_vector_type(2)));
typedef __amdgpu_buffer_rsrc_t rsrc_t;

constexpr int SEQ = 2048, DM = 640, NMODE = 128;
constexpr unsigned XBYTES = SEQ * DM * 2, MBYTES = DM * DM * 2, SBYTES = DM * NMODE * 2, VBYTES = DM * 2;

constexpr unsigned WS_EPOCH = 0;
constexpr unsigned WS_FLAG = 4096;
constexpr unsigned FL_H = 0, FL_XT = 160, FL_Y = 240, FL_G = 320, FL_N = 336;
constexpr unsigned WS_G = 96 * 1024;
constexpr unsigned WS_AGG = 128 * 1024;
constexpr unsigned WS_H = 512 * 1024;
constexpr unsigned WS_XT = WS_H + SEQ * 1280 * 2;
constexpr unsigned WS_Y = WS_XT + XBYTES;
constexpr unsigned WS_END = WS_Y + XBYTES;
constexpr unsigned WS_STAMP = 16u << 20;

constexpr int LDS_BYTES = 163840;
constexpr unsigned LDS_DUMMY = LDS_BYTES - 256;
constexpr int SPIN_CAP = 20000;
constexpr int AUX_SC1 = 16;

#define NC1 20
#define NC2 10

struct Params {
    const void *x, *lre, *lim, *bre, *bim, *cre, *cim, *ldt, *dska;
    const void *sare, *saim, *sbre, *sbim, *scre, *scim, *sldt, *dskb, *gw;
    void *out, *ws;
};

DI rsrc_t mk(const void* p, unsigned bytes) { return __builtin_amdgcn_make_buffer_rsrc((void*)p, (short)0, (int)bytes, 0x00020000); }
DI float bflo(unsigned v) { return __uint_as_float(v << 16); }
DI float bfhi(unsigned v) { return __uint_as_float(v & 0xffff0000u); }
DI unsigned pk2(float a, float b) { f32x2 v = {a, b}; return __builtin_bit_cast(unsigned, __builtin_convertvector(v, bf16x2)); }
DI unsigned pk2v(f32x2 v) { return __builtin_bit_cast(unsigned, __builtin_convertvector(v, bf16x2)); }
DI float rbf(float a) { return bflo(pk2(a, a)); }
DI unsigned rfl(unsigned v) { return __builtin_amdgcn_readfirstlane(v); }
#define SB() __builtin_amdgcn_sched_barrier(0)
#define MEMFENCE() asm volatile("" ::: "memory")

template <class T> DI T lds_ld(unsigned a) { return *(const LAS T*)(uintptr_t)a; }
template <class T> DI void lds_st(unsigned a, T v) { *(LAS T*)(uintptr_t)a = v; }
DI v2u lds_tr(unsigned a) { return __builtin_bit_cast(v2u, __builtin_amdgcn_ds_read_tr16_b64_v4i16((LAS s16x4*)(uintptr_t)a)); }

#define MFMA32(acc, a, b) asm volatile("v_mfma_f32_32x32x16_bf16 %0, %1, %2, %0" : "+v"(acc) : "v"(a), "v"(b))
#define MFMA32Z(acc, a, b) asm volatile("v_mfma_f32_32x32x16_bf16 %0, %1, %2, 0" : "=&v"(acc) : "v"(a), "v"(b))
#define MFMA16A(acc, a, b) asm volatile("v_mfma_f32_16x16x32_bf16 %0, %1, %2, %0" : "+v"(acc) : "a"(a), "v"(b))
#define MFMA16AZ(acc, a, b) asm volatile("v_mfma_f32_16x16x32_bf16 %0, %1, %2, 0" : "=&v"(acc) : "a"(a), "v"(b))

DI void dma16(unsigned ldsoff, unsigned voff, rsrc_t rs, unsigned soff) {
    asm volatile("s_mov_b32 m0, %0\n\ts_nop 0\n\tbuffer_load_dwordx4 %1, %2, %3 offen lds" ::"s"(ldsoff), "v"(voff), "s"(rs), "s"(soff) : "memory");
}
DI void touch(unsigned voff, rsrc_t rs, unsigned soff) {
    asm volatile("s_mov_b32 m0, %0\n\ts_nop 0\n\tbuffer_load_dword %1, %2, %3 offen lds" ::"s"(LDS_DUMMY), "v"(voff), "s"(rs), "s"(soff) : "memory");
}
#define WAITV(n) asm volatile("s_waitcnt vmcnt(%0)" ::"n"(n) : "memory")
#define BARRIER() asm volatile("s_waitcnt lgkmcnt(0)\n\ts_barrier" ::: "memory")

#define STAMP(i) do { SB(); if (w == 0) { u64 t_, c_; asm volatile("s_memrealtime %0\n\ts_memtime %1\n\ts_waitcnt lgkmcnt(0)" : "=s"(t_), "=s"(c_)::"memory"); \
    lds_st<v4u>(163328 + (i) * 16, v4u{(unsigned)t_, (unsigned)(t_ >> 32), (unsigned)c_, (unsigned)(c_ >> 32)}); asm volatile("s_waitcnt lgkmcnt(0)" ::: "memory"); } SB(); } while (0)

DI void poll_flags(rsrc_t ws, unsigned off, unsigned tA, unsigned tB) {
    for (int spin = 0; spin < SPIN_CAP; ++spin) {
        MEMFENCE();
        const v2u f = __builtin_amdgcn_raw_buffer_load_b64(ws, off, 0, AUX_SC1);
        if (__all((f.x == tA) & (f.y == tB))) break;
    }
    __builtin_amdgcn_fence(__ATOMIC_ACQUIRE, "wavefront");
    MEMFENCE();
}
DI void set_flag(rsrc_t ws, unsigned idx, unsigned tA, unsigned tB) {
    if (threadIdx.x == 0) __builtin_amdgcn_raw_buffer_store_b64(v2u{tA, tB}, ws, WS_FLAG + idx * 256, 0, AUX_SC1);
}

struct cpx { float r, i; };
DI cpx cmul(cpx a, cpx b) { return {a.r * b.r - a.i * b.i, a.r * b.i + a.i * b.r}; }
DI cpx cfma(cpx a, cpx b, cpx c) { return {a.r * b.r - a.i * b.i + c.r, a.r * b.i + a.i * b.r + c.i}; }
struct cp2 { f32x2 r, i; };
DI cp2 cmul2(cp2 a, cp2 b) { return {a.r * b.r - a.i * b.i, a.r * b.i + a.i * b.r}; }
DI cp2 cmul2s(cp2 a, cpx b) { return {a.r * b.r - a.i * b.i, a.r * b.i + a.i * b.r}; }
DI void halves(float v, float& lo, float& hi) {
    auto s = __builtin_amdgcn_permlane32_swap(__float_as_uint(v), __float_as_uint(v), false, false);
    lo = __uint_as_float(s[0]); hi = __uint_as_float(s[1]);
}

struct CStage { rsrc_t r0, r1; unsigned vo[2], ra[2]; int w; };
DI void c_dma(const CStage& c, int pb, int i, unsigned buf) {
    const unsigned u = 5u * c.w + pb;
    dma16(buf + i * 1024, c.vo[i & 1], (pb & 1) ? c.r1 : c.r0, (u >> 1) * 128u + i * 8u * 1280u);
}
DI void c_read(const CStage& c, v4u (&cf)[50], int pb, int f, unsigned buf) {
    cf[10 * pb + f] = lds_ld<v4u>(buf + c.ra[f / 5] + (f % 5) * 2048);
}

struct KgIn { unsigned are, aim, bre, bim, cre, cim, ldt; };
DI cp2 cexp2(f32x2 zr2, f32x2 zi2, float l) {
    const f32x2 a = zr2 * l, b = zi2 * l;
    cp2 o;
    const float m0 = __builtin_amdgcn_exp2f(a.x), m1 = __builtin_amdgcn_exp2f(a.y);
    const float f0 = __builtin_amdgcn_fractf(b.x), f1 = __builtin_amdgcn_fractf(b.y);
    o.r = f32x2{m0 * __builtin_amdgcn_cosf(f0), m1 * __builtin_amdgcn_cosf(f1)};
    o.i = f32x2{m0 * __builtin_amdgcn_sinf(f0), m1 * __builtin_amdgcn_sinf(f1)};
    return o;
}
constexpr unsigned STG_ROW = 528, STG_BYTES = 53 * STG_ROW, KEXP_BASE = 4 * STG_BYTES, KEXP_RHO = 1216, KEXP_CH = 4 * KEXP_RHO;
constexpr unsigned U_CH = 4160, YST_OFF = 2 * U_CH, TAB_OFF = YST_OFF + 8192;
DI void put_lo(unsigned a, int row, cp2 v) { lds_st<v2u>(a + (21 + row) * STG_ROW, v2u{pk2v(v.r), pk2v(v.i)}); }
DI void put_hi(unsigned a, int row, cp2 v) { lds_st<v2u>(a + row * STG_ROW, v2u{pk2v(v.r), pk2v(-v.i)}); }

struct KgState { f32x2 zr2, zi2; cp2 w; };
DI KgState kg_lo(const KgIn& in, unsigned stg, int lane) {
    const float dt = rbf(__expf(bflo(in.ldt)));
    KgState s;
    const f32x2 zr = f32x2{bflo(in.are), bfhi(in.are)} * dt, zi = f32x2{bflo(in.aim), bfhi(in.aim)} * dt;
    s.zr2 = zr * 1.44269504088896341f;
    s.zi2 = zi * 0.15915494309189535f;
    const cp2 db = {f32x2{bflo(in.bre), bfhi(in.bre)} * dt, f32x2{bflo(in.bim), bfhi(in.bim)} * dt};
    const cp2 cc = {f32x2{bflo(in.cre), bfhi(in.cre)}, f32x2{bflo(in.cim), bfhi(in.cim)}};
    s.w = cmul2(cc, db);
    const unsigned a = stg + lane * 8;
    const cp2 e8 = cexp2(s.zr2, s.zi2, 8.f), e32 = cexp2(s.zr2, s.zi2, 32.f);
    cp2 v0 = {f32x2{1.f, 1.f}, f32x2{0.f, 0.f}}, v1 = e8, v2 = cmul2(e8, e8), v3 = cmul2(v2, e8);
    put_lo(a, 0, v0); put_lo(a, 1, v1); put_lo(a, 2, v2); put_lo(a, 3, v3);
    v0 = e32;
#pragma unroll
    for (int k = 1; k < 8; ++k) {
        if (k > 1) v0 = cmul2(v0, e32);
        v1 = cmul2(v1, e32); v2 = cmul2(v2, e32); v3 = cmul2(v3, e32);
        put_lo(a, 4 * k, v0); put_lo(a, 4 * k + 1, v1); put_lo(a, 4 * k + 2, v2); put_lo(a, 4 * k + 3, v3);
    }
    return s;
}
DI void kg_hi(const KgState& s, unsigned stg, int lane) {
    const unsigned a = stg + lane * 8;
    const cp2 e1 = cexp2(s.zr2, s.zi2, 1.f), e256 = cexp2(s.zr2, s.zi2, 256.f);
    const cp2 e2 = cmul2(e1, e1), e4 = cmul2(e2, e2);
    cp2 w0 = s.w, w1 = cmul2(w0, e1), w2 = cmul2(w0, e2), w3 = cmul2(w1, e2);
    put_hi(a, 0, w0); put_hi(a, 1, w1); put_hi(a, 2, w2); put_hi(a, 3, w3);
    put_hi(a, 4, cmul2(w0, e4)); put_hi(a, 5, cmul2(w1, e4)); put_hi(a, 6, cmul2(w2, e4)); put_hi(a, 7, cmul2(w3, e4));
    cp2 q = cmul2(w0, e256);
    const cp2 q2 = cmul2(q, e2);
    put_hi(a, 8, q); put_hi(a, 9, q2); put_hi(a, 10, cmul2(q, e4)); put_hi(a, 11, cmul2(q2, e4));
    q = cmul2(q, e256); put_hi(a, 12, q); put_hi(a, 13, cmul2(q, e4));
    q = cmul2(q, e256); put_hi(a, 14, q); put_hi(a, 15, cmul2(q, e4));
#pragma unroll
    for (int m = 0; m < 5; ++m) { q = cmul2(q, e256); put_hi(a, 16 + m, q); }
}
DI void kg_finish(unsigned stg, unsigned kexp, int lane, const unsigned (&ga)[32]) {
    const int j = lane & 31, hh = lane >> 5;
    const unsigned ra = stg + j * STG_ROW + hh * 16;
    f32x16 d;
#pragma unroll
    for (int ks = 0; ks < 16; ++ks) {
        const v4u a = lds_ld<v4u>(ra + ks * 32), b = lds_ld<v4u>(ra + 21 * STG_ROW + ks * 32);
        if (ks == 0) MFMA32Z(d, a, b); else MFMA32(d, a, b);
    }
    asm volatile("s_nop 11" : "+v"(d));
    const unsigned tab = stg + TAB_OFF;
    lds_st<v2u>(tab + (8 * j + 4 * hh) * 2, v2u{pk2(d[0], d[1]), pk2(d[2], d[3])});
    const unsigned a1 = tab + (hh ? 384 + 2 * j : 256 + 4 * j) * 2, a2 = tab + (hh ? 448 + 2 * j : 258 + 4 * j) * 2;
    lds_st<unsigned>(a1, pk2(d[4], d[5]));
    lds_st<unsigned>(a2, pk2(d[6], d[7]));
    const unsigned a3 = tab + (hh ? 640 + j : 512 + j) * 2;
    const unsigned p89 = pk2(d[8], d[9]), pab = pk2(d[10], d[11]);
    lds_st<u16>(a3, (u16)p89); lds_st<u16>(a3 + 64, (u16)(p89 >> 16));
    lds_st<u16>(a3 + 128, (u16)pab); lds_st<u16>(a3 + 192, (u16)(pab >> 16));
    const unsigned wa = kexp + (lane & 3) * KEXP_RHO + (lane >> 2) * 16;
#pragma unroll
    for (int r = 0; r < 4; ++r) {
        v4u o;
#pragma unroll
        for (int e = 0; e < 4; ++e) {
            const unsigned lo = lds_ld<u16>(ga[8 * r + 2 * e]), hi = lds_ld<u16>(ga[8 * r + 2 * e + 1]);
            o[e] = lo | (hi << 16);
        }
        lds_st<v4u>(wa + r * 256, o);
    }
    if (lane < 28) lds_st<v4u>(wa + 1024, v4u{0, 0, 0, 0});
}

#define SHADD(d, prev, cur, y) asm volatile("v_add_f32_dpp %0, %1, %3 wave_ror:1 row_mask:0xf bank_mask:0xf\n\t" \
    "v_add_f32_dpp %0, %2, %3 wave_shr:1 row_mask:0xf bank_mask:0xf" : "=&v"(d) : "v"(prev), "v"(cur), "v"(y))
#define SHADD0(d, cur, y) asm volatile("v_add_f32_dpp %0, %1, %2 wave_shr:1 row_mask:0xf bank_mask:0xf bound_ctrl:0" : "=&v"(d) : "v"(cur), "v"(y))

template <int E>
DI void conv_channel(unsigned kexp, unsigned ub, unsigned yst, int lane, const CStage& cs, v4u (&cf)[50], unsigned cbuf) {
    const int hh = lane >> 5;
    const unsigned aA = kexp + (lane & 3) * KEXP_RHO + (56 - 8 * ((lane & 31) >> 2) + 8 * hh) * 2;
    const unsigned aB = ub + 2 * (4 + 32 * hh + 4 * ((lane >> 2) & 3) + 16 * ((lane >> 4) & 1) + 4 * (lane & 3));
    f32x16 acc[8];
    v4u af[2][8];
#pragma unroll
    for (int dl = 0; dl < 8; ++dl) af[0][dl] = lds_ld<v4u>(aA + (64 * (7 - dl)) * 2);
    v2u b0 = lds_tr(aB), b1 = lds_tr(aB + 32);
#pragma unroll
    for (int c = 0; c < 4; ++c) {
#pragma unroll
        for (int i = 0; i < 8; ++i) {
            const v4u bf = {b0.x, b0.y, b1.x, b1.y};
            const int ni = (i + 1) & 7, nc = c + (i == 7);
            if (nc < 4) { b0 = lds_tr(aB + 128 * (4 * ni + nc)); b1 = lds_tr(aB + 128 * (4 * ni + nc) + 32); }
            if (c < 3) af[(c + 1) & 1][i] = lds_ld<v4u>(aA + (64 * (7 - i) + 16 * (c + 1)) * 2);
            SB();
#pragma unroll
            for (int dl = 0; dl < 8 - i; ++dl) {
                if (c == 0 && i == 0) MFMA32Z(acc[dl], af[0][dl], bf);
                else MFMA32(acc[i + dl], af[c & 1][dl], bf);
                const int m = 36 * c + 8 * i - i * (i - 1) / 2 + dl, pb = 2 * E + m / 72, mm = m % 72;
                if (mm < 40 && (mm & 3) == 0) { SB(); c_dma(cs, pb, mm >> 2, cbuf); SB(); }
                if (mm == 59) { SB(); WAITV(0); SB(); }
                if (mm >= 60 && mm < 70) { SB(); c_read(cs, cf, pb, mm - 60, cbuf); SB(); }
                if (mm == 71) { SB(); asm volatile("s_waitcnt lgkmcnt(0)" ::: "memory"); SB(); }
            }
            SB();
        }
    }
    asm volatile("s_nop 11" : "+v"(acc[7]));
    float p2[32], p1[32], y[32];
#pragma unroll
    for (int G = 0; G < 32; ++G) {
        const float cur = acc[G >> 2][4 * (G & 3) + 3], yy = acc[G >> 2][4 * (G & 3) + 2];
        if (G == 0) SHADD0(p2[0], cur, yy);
        else { const float prev = acc[(G - 1) >> 2][4 * ((G - 1) & 3) + 3]; SHADD(p2[G], prev, cur, yy); }
    }
#pragma unroll
    for (int G = 0; G < 32; ++G) {
        const float yy = acc[G >> 2][4 * (G & 3) + 1];
        if (G == 0) SHADD0(p1[0], p2[0], yy); else SHADD(p1[G], p2[G - 1], p2[G], yy);
    }
#pragma unroll
    for (int G = 0; G < 32; ++G) {
        const float yy = acc[G >> 2][4 * (G & 3)];
        if (G == 0) SHADD0(y[0], p1[0], yy); else SHADD(y[G], p1[G - 1], p1[G], yy);
    }
    const unsigned ya = yst + lane * 4;
#pragma unroll
    for (int G = 0; G < 32; G += 2) {
        const unsigned pk = pk2(y[G], y[G + 1]);
        lds_st<u16>(ya + G * 256, (u16)pk);
        lds_st<u16>(ya + G * 256 + 256, (u16)(pk >> 16));
    }
}

DI unsigned red_off(int i) { return i * 5120 + (i >= 6 ? 43008 : 0) + (i >= 10 ? 45056 : 0); }

__global__ __launch_bounds__(256, 1) void k_fused(Params p) {
    const int tid = threadIdx.x, lane = tid & 63;
    const int w = __builtin_amdgcn_readfirstlane(tid >> 6);
    const int b = blockIdx.x, xl = b & 7, r = b >> 3;
    if (tid < 16) lds_st<v4u>(163328 + tid * 16, v4u{0, 0, 0, 0});
    STAMP(0);
    const rsrc_t ws = mk(p.ws, WS_END);
    const rsrc_t rx = mk(p.x, XBYTES);
    const int rn = r < 20 ? r : r - 20;
    const int tti = rn & 3, kk5 = rn >> 2;
    const int ct = (2 * tti + (r < 20 ? (kk5 < 2 ? kk5 + 2 : kk5 + 3) : (kk5 < 2 ? kk5 : 4))) & 7;
    const int tt = 4 * xl + tti;
    CStage cs;
    cs.r0 = mk((w & 1) ? p.cim : p.cre, MBYTES); cs.r1 = mk((w & 1) ? p.cre : p.cim, MBYTES); cs.w = w;
#pragma unroll
    for (int e = 0; e < 2; ++e) {
        cs.vo[e] = (80 * ct + (lane >> 3)) * 1280 + (((lane & 7) ^ ((4 * e + (lane >> 4)) & 7)) * 16);
        cs.ra[e] = (lane & 15) * 128 + (((4 * e + (lane >> 4)) ^ ((lane >> 1) & 7)) * 16);
    }
    v4u cf[50];
    unsigned tA, tB, cbuf3, cbuf4;

    if (r < 20) {
        const int c = 4 * (xl & 3) + (r & 3), g = 5 * (xl >> 2) + (r >> 2);
        const int th = w & 1, cg = w >> 1, ch = lane & 31, hh = lane >> 5;
        const rsrc_t rbre = mk(p.bre, MBYTES), rbim = mk(p.bim, MBYTES);
        const int pch = 64 * g + 32 * cg + ch;
        unsigned vlr, vli, vld; v2u ep;
        {
            const rsrc_t r0 = mk(p.lre, VBYTES), r1 = mk(p.lim, VBYTES), r2 = mk(p.ldt, VBYTES);
            asm volatile("s_nop 4\n\tbuffer_load_ushort %0, %4, %5, 0 offen\n\tbuffer_load_ushort %1, %4, %6, 0 offen\n\t"
                         "buffer_load_ushort %2, %4, %7, 0 offen\n\tbuffer_load_dwordx2 %3, off, %8, 0 sc1"
                         : "=&v"(vlr), "=&v"(vli), "=&v"(vld), "=&v"(ep) : "v"(pch * 2), "s"(r0), "s"(r1), "s"(r2), "s"(ws) : "memory");
        }
        const int rb = lane >> 3;
        const unsigned gch = ((lane & 7) ^ ((4 * w + (rb >> 1)) & 7)) * 16;
        const unsigned vox = (32 * (rb >> 2) + (rb & 3)) * 1280 + gch, vob = rb * 1280 + gch;
        const unsigned sox = (128 * c + 4 * w) * 1280, sob = (64 * g + 8 * w) * 1280;
        auto dma_piece = [&](int ti, unsigned slot, unsigned ko) {
            const unsigned l = slot + ti * 4096 + w * 1024;
            if (ti < 4) dma16(l, vox, rx, sox + (64 * (ti >> 1) + 16 * (ti & 1)) * 1280 + ko);
            else dma16(l, vob, (ti & 1) ? rbim : rbre, sob + 32 * ((ti - 4) >> 1) * 1280 + ko);
        };
#pragma unroll
        for (int ti = 0; ti < 8; ++ti) dma_piece(ti, 0, 0);
        asm volatile("s_waitcnt vmcnt(8)" : "+v"(vlr), "+v"(vli), "+v"(vld), "+v"(ep)::"memory");
        tA = rfl(ep.x) + 1; tB = rfl(ep.y) + 1;
        cpx lam, S2;
        {
            const float dt = rbf(__expf(bflo(vld))), Lr = bflo(vlr), Li = bflo(vli);
            const float e = __expf(dt * Lr), fr = __builtin_amdgcn_fractf((dt * Li) * 0.15915494309189535f);
            lam = {e * __builtin_amdgcn_cosf(fr), e * __builtin_amdgcn_sinf(fr)};
            const float nr = lam.r - 1.f, ni = lam.i, den = 2.f * __builtin_amdgcn_rcpf(Lr * Lr + Li * Li);
            S2 = {(nr * Lr + ni * Li) * den, (ni * Lr - nr * Li) * den};
        }
        cp2 Q[16];
        Q[0] = {f32x2{1.f, lam.r}, f32x2{0.f, lam.i}};
        const cpx l2 = cmul(lam, lam), l4 = cmul(l2, l2), l8 = cmul(l4, l4), l16 = cmul(l8, l8), l32 = cmul(l16, l16);
        SB(); dma_piece(0, 32768, 128); dma_piece(1, 32768, 128); SB();
        Q[1] = cmul2s(Q[0], l2);
#pragma unroll
        for (int i = 0; i < 2; ++i) Q[2 + i] = cmul2s(Q[i], l4);
        SB(); dma_piece(2, 32768, 128); SB();
#pragma unroll
        for (int i = 0; i < 4; ++i) Q[4 + i] = cmul2s(Q[i], l8);
        SB(); dma_piece(3, 32768, 128); SB();
#pragma unroll
        for (int i = 0; i < 8; ++i) { Q[8 + i] = cmul2s(Q[i], l16); if ((i & 1) == 1) { SB(); dma_piece(4 + (i >> 1), 32768, 128); SB(); } }
        const cpx l64 = cmul(l32, l32), l128 = cmul(l64, l64), l256 = cmul(l128, l128), l512 = cmul(l256, l256);
        const int seg = 2 * th + hh;
        cpx lseg = {1.f, 0.f}, lgat = {1.f, 0.f};
        if (hh) { lseg = l32; lgat = l128; }
        if (th) { lseg = cmul(lseg, l64); lgat = cmul(lgat, l256); }
        const cpx Sl = cmul(S2, lam);

        f32x16 aR[2], aI[2];
#pragma unroll
        for (int i = 0; i < 16; ++i) { aR[0][i] = 0.f; aR[1][i] = 0.f; aI[0][i] = 0.f; aI[1][i] = 0.f; }
        unsigned adA[4], adB[4];
        {
            const unsigned lb = ch * 128 + ((hh ^ ((ch >> 1) & 7)) * 16);
#pragma unroll
            for (int k = 0; k < 4; ++k) { adA[k] = (lb ^ (k * 32)) + th * 8192; adB[k] = (lb ^ (k * 32)) + 16384 + cg * 8192; }
        }
        STAMP(1);
        asm volatile("s_waitcnt vmcnt(8)\n\ts_barrier" : "+v"(aR[0]), "+v"(aI[0]), "+v"(aR[1]), "+v"(aI[1])::"memory");
        v4u fa0 = lds_ld<v4u>(adA[0]), fa1 = lds_ld<v4u>(adA[0] + 4096), fr = lds_ld<v4u>(adB[0]), fi = lds_ld<v4u>(adB[0] + 4096);
        const unsigned cb0 = 98304 + w * 10240, cb1 = 32768 + w * 10240;
#pragma unroll
        for (int s = 0; s < 10; ++s) {
#pragma unroll
            for (int k = 0; k < 4; ++k) {
                v4u na0 = fa0, na1 = fa1, nr = fr, ni = fi;
                if (k == 3) {
                    if (s <= 8) asm volatile("s_waitcnt vmcnt(6) lgkmcnt(0)\n\ts_barrier" ::: "memory");
                }
                const int kn = (k + 1) & 3;
                const unsigned so = ((k == 3 ? s + 1 : s) % 3) * 32768;
                const bool rd = !(k == 3 && s == 9);
                MFMA32(aR[0], fa0, fr); SB();
                if (rd) { na0 = lds_ld<v4u>(adA[kn] + so); nr = lds_ld<v4u>(adB[kn] + so); } SB();
                MFMA32(aI[0], fa0, fi); SB();
                if (rd) { ni = lds_ld<v4u>(adB[kn] + so + 4096); na1 = lds_ld<v4u>(adA[kn] + so + 4096); } SB();
                MFMA32(aR[1], fa1, fr);
                if (s <= 7) dma_piece(2 * k, ((s + 2) % 3) * 32768, (s + 2) * 128);
                else if (s == 8) c_dma(cs, 0, 2 * k, cb0);
                else if (k == 0) c_dma(cs, 0, 8, cb0);
                MFMA32(aI[1], fa1, fi);
                if (s <= 7) dma_piece(2 * k + 1, ((s + 2) % 3) * 32768, (s + 2) * 128);
                else if (s == 8) c_dma(cs, 0, 2 * k + 1, cb0);
                else if (k == 0) c_dma(cs, 0, 9, cb0);
                SB();
                fa0 = na0; fa1 = na1; fr = nr; fi = ni;
            }
        }
        asm volatile("s_nop 11" : "+v"(aR[0]), "+v"(aI[0]), "+v"(aR[1]), "+v"(aI[1]));
        STAMP(2);
        cpx agg;
        {
            f32x2 sr0 = {0.f, 0.f}, si0 = {0.f, 0.f}, sr1 = {0.f, 0.f}, si1 = {0.f, 0.f};
#pragma unroll
            for (int i = 0; i < 8; ++i) {
                const f32x2 ur = {aR[0][2 * i], aR[0][2 * i + 1]}, ui = {aI[0][2 * i], aI[0][2 * i + 1]};
                const f32x2 qr = Q[15 - i].r.yx, qi = Q[15 - i].i.yx;
                sr0 += qr * ur; si0 += qr * ui; sr0 -= qi * ui; si0 += qi * ur;
                const f32x2 vr = {aR[1][2 * i], aR[1][2 * i + 1]}, vi = {aI[1][2 * i], aI[1][2 * i + 1]};
                const f32x2 pr = Q[7 - i].r.yx, pi = Q[7 - i].i.yx;
                sr1 += pr * vr; si1 += pr * vi; sr1 -= pi * vi; si1 += pi * vr;
            }
            sr0 += sr1; si0 += si1;
            agg = {sr0.x + sr0.y, si0.x + si0.y};
        }
        cpx a0, a1;
        halves(agg.r, a0.r, a1.r); halves(agg.i, a0.i, a1.i);
        const cpx a64 = cfma(l32, a0, a1);
        const unsigned xch = 159744 + (cg * 2) * 256 + ch * 8;
        if (hh == 0) lds_st<f32x2>(xch + th * 256, f32x2{a64.r, a64.i});
        BARRIER();
        cpx pre = {0.f, 0.f};
        if (th) {
            const f32x2 o = lds_ld<f32x2>(xch);
            const cpx o64 = {o.x, o.y};
            const cpx a128 = cfma(l64, o64, a64);
            if (hh == 0 && c < 15)
                __builtin_amdgcn_raw_buffer_store_b128(v4u{__float_as_uint(a128.r), tA, __float_as_uint(a128.i), tB}, ws,
                                                       WS_AGG + (c * 640 + pch) * 16, 0, AUX_SC1);
            pre = hh ? cfma(l32, o64, a0) : o64;
        } else if (hh) pre = a0;
        STAMP(3);
        {
            cpx v = {0.f, 0.f};
#pragma unroll
            for (int t = 0; t < 32; ++t) {
                v = cfma(lam, v, cpx{aR[t >> 4][t & 15], aI[t >> 4][t & 15]});
                const cpx o = cmul(S2, v);
                aR[t >> 4][t & 15] = o.r; aI[t >> 4][t & 15] = o.i;
                SB();
                if (t < 10) c_dma(cs, 1, t, cb1);
                if (t == 10) { if (th && c < 15) WAITV(11); else WAITV(10); }
                if (t >= 10 && t < 20) c_read(cs, cf, 0, t - 10, cb0);
                if (t == 21) asm volatile("s_waitcnt lgkmcnt(0)" ::: "memory");
                if (t >= 21 && t < 31) c_dma(cs, 2, t - 21, cb0);
                SB();
            }
        }
        asm volatile("" : "+v"(aR[0]), "+v"(aI[0]), "+v"(aR[1]), "+v"(aI[1]));
        cpx carry = {0.f, 0.f};
        if (c > 0) {
            v4u ga[4];
            for (int spin = 0; spin < SPIN_CAP; ++spin) {
                MEMFENCE();
                bool ok = true;
#pragma unroll
                for (int i = 0; i < 4; ++i) {
                    const int j = c - 1 - (4 * i + seg);
                    ga[i] = __builtin_amdgcn_raw_buffer_load_b128(ws, WS_AGG + ((j < 0 ? 0 : j) * 640 + pch) * 16, 0, AUX_SC1);
                }
#pragma unroll
                for (int i = 0; i < 4; ++i) ok = ok & ((c - 1 - (4 * i + seg) < 0) | ((ga[i].y == tA) & (ga[i].w == tB)));
                if (__all(ok)) break;
            }
            cpx part = {0.f, 0.f};
#pragma unroll
            for (int i = 3; i >= 0; --i) {
                const bool val = c - 1 - (4 * i + seg) >= 0;
                const cpx a = {val ? __uint_as_float(ga[i].x) : 0.f, val ? __uint_as_float(ga[i].z) : 0.f};
                part = cfma(l512, part, a);
            }
            part = cmul(lgat, part);
            cpx p0, p1;
            halves(part.r, p0.r, p1.r); halves(part.i, p0.i, p1.i);
            const cpx ps = {p0.r + p1.r, p0.i + p1.i};
            if (hh == 0) lds_st<f32x2>(xch + 1024 + th * 256, f32x2{ps.r, ps.i});
            BARRIER();
            const f32x2 o = lds_ld<f32x2>(xch + 1024 + (th ^ 1) * 256);
            carry = {ps.r + o.x, ps.i + o.y};
        }
        STAMP(4);
        const cpx F = cmul(Sl, cfma(lseg, carry, pre));
        if (c == 0) WAITV(0);
        {
            const unsigned hs = w * 8192;
            const unsigned gsw = ((ch >> 1) & 1) << 2 | ((ch >> 3) & 1) << 3 | (ch & 1) | ((ch >> 2) & 1) << 1;
            const unsigned wb = hs + ch * 128;
#pragma unroll
            for (int q = 0; q < 8; ++q) {
                f32x2 hr[2], hi[2];
#pragma unroll
                for (int e = 0; e < 2; ++e) {
                    const int t = 4 * q + 2 * e;
                    const f32x2 ur = {aR[t >> 4][t & 15], aR[t >> 4][(t & 15) + 1]}, ui = {aI[t >> 4][t & 15], aI[t >> 4][(t & 15) + 1]};
                    const cp2 qq = Q[t >> 1];
                    hr[e] = ur + qq.r * F.r - qq.i * F.i;
                    hi[e] = -ui - qq.r * F.i - qq.i * F.r;
                }
                const unsigned a = wb + (((8 * hh + q) ^ gsw) * 8);
                lds_st<v2u>(a, v2u{pk2v(hr[0]), pk2v(hr[1])});
                lds_st<v2u>(a + 4096, v2u{pk2v(hi[0]), pk2v(hi[1])});
            }
            const int gq = lane >> 4, ra = (lane >> 2) & 3, rp = lane & 3;
            unsigned rad[2];
#pragma unroll
            for (int hf = 0; hf < 2; ++hf) {
                const int row = 8 * gq + 4 * hf + ra;
                const unsigned gs = ((row >> 1) & 1) << 2 | ((row >> 3) & 1) << 3 | (row & 1) | ((row >> 2) & 1) << 1;
                rad[hf] = hs + row * 128 + ((rp ^ gs) * 8);
            }
            const unsigned hso = WS_H + ((2 * c + th) * 40 + 4 * g + cg) * 4096;
            v4u hq[8];
#pragma unroll
            for (int i = 0; i < 8; ++i) {
                const v2u x0 = lds_tr((rad[0] ^ ((i & 3) * 32)) + (i >> 2) * 4096), x1 = lds_tr((rad[1] ^ ((i & 3) * 32)) + (i >> 2) * 4096);
                hq[i] = v4u{x0.x, x0.y, x1.x, x1.y};
            }
            SB();
#pragma unroll
            for (int i = 0; i < 8; ++i)
                __builtin_amdgcn_raw_buffer_store_b128(hq[i], ws, lane * 16 + (i & 3) * 1024, hso + (i >> 2) * 8192, AUX_SC1);
            SB();
        }
#pragma unroll
        for (int f = 0; f < 10; ++f) { c_read(cs, cf, 1, f, cb1); c_read(cs, cf, 2, f, cb0); }
        asm volatile("s_waitcnt lgkmcnt(0)" ::: "memory");
#pragma unroll
        for (int i = 0; i < 10; ++i) c_dma(cs, 3, i, cb1);
        asm volatile("s_waitcnt vmcnt(10) lgkmcnt(0)\n\ts_barrier" ::: "memory");
        set_flag(ws, FL_H + c * 10 + g, tA, tB);
        STAMP(5);
        MEMFENCE();
#pragma unroll
        for (int i = 0; i < 10; ++i) c_dma(cs, 4, i, cb0);
        cbuf3 = cb1; cbuf4 = cb0;
    } else if (r < 30) {
        const int jj = r - 20, grp = xl * 10 + jj, H = xl >> 2, tb = 2 * (xl & 3) + H;
        const unsigned stg = w * STG_BYTES, kexp = KEXP_BASE + w * 2 * KEXP_CH;
        KgIn in[2]; v2u ep;
        {
            const rsrc_t r0 = mk(p.sare, SBYTES), r1 = mk(p.saim, SBYTES), r2 = mk(p.sbre, SBYTES), r3 = mk(p.sbim, SBYTES),
                         r4 = mk(p.scre, SBYTES), r5 = mk(p.scim, SBYTES), r6 = mk(p.sldt, VBYTES);
            const unsigned d0 = grp * 8 + 2 * w;
#pragma unroll
            for (int e = 0; e < 2; ++e)
                asm volatile("s_nop 4\n\tbuffer_load_dword %0, %7, %8, %15 offen\n\tbuffer_load_dword %1, %7, %9, %15 offen\n\t"
                             "buffer_load_dword %2, %7, %10, %15 offen\n\tbuffer_load_dword %3, %7, %11, %15 offen\n\t"
                             "buffer_load_dword %4, %7, %12, %15 offen\n\tbuffer_load_dword %5, %7, %13, %15 offen\n\t"
                             "buffer_load_ushort %6, off, %14, %16"
                             : "=&v"(in[e].are), "=&v"(in[e].aim), "=&v"(in[e].bre), "=&v"(in[e].bim), "=&v"(in[e].cre), "=&v"(in[e].cim), "=&v"(in[e].ldt)
                             : "v"(lane * 4), "s"(r0), "s"(r1), "s"(r2), "s"(r3), "s"(r4), "s"(r5), "s"(r6), "s"((d0 + e) * 256), "s"((d0 + e) * 2)
                             : "memory");
            asm volatile("buffer_load_dwordx2 %0, off, %1, 0 sc1" : "=&v"(ep) : "s"(ws) : "memory");
        }
        {
            const int rb = lane >> 3;
#pragma unroll
            for (int i = 0; i < 8; ++i) {
                const unsigned f = (((rb >> 1) & 1) | ((i & 1) << 1)) << 1;
                dma16(kexp + i * 1024, rb * 1280 + (((lane & 7) ^ f) * 16), rx, (256 * tb + 64 * w + 8 * i) * 1280 + jj * 128);
            }
        }
        const rsrc_t rpf = mk(jj < 5 ? p.bre : p.bim, MBYTES);
        const unsigned pfo = (320 * H + 64 * (jj < 5 ? jj : jj - 5)) * 1280;
        touch(lane * 1280, rpf, pfo + w * 128);
        asm volatile("s_waitcnt vmcnt(9)" : "+v"(in[0].are), "+v"(in[0].aim), "+v"(in[0].bre), "+v"(in[0].bim), "+v"(in[0].cre), "+v"(in[0].cim), "+v"(in[0].ldt),
                     "+v"(in[1].are), "+v"(in[1].aim), "+v"(in[1].bre), "+v"(in[1].bim), "+v"(in[1].cre), "+v"(in[1].cim), "+v"(in[1].ldt), "+v"(ep)::"memory");
        tA = rfl(ep.x) + 1; tB = rfl(ep.y) + 1;
        STAMP(1);
        KgState ks = kg_lo(in[0], stg, lane);
        WAITV(1);
        {
            const int g4 = lane >> 4, ra = (lane >> 2) & 3, rp = lane & 3;
            v4u xq[8];
#pragma unroll
            for (int tk = 0; tk < 2; ++tk)
#pragma unroll
                for (int cs = 0; cs < 4; ++cs) {
                    v2u x[2];
#pragma unroll
                    for (int hf = 0; hf < 2; ++hf) {
                        const int row = 32 * tk + 8 * g4 + 4 * hf + ra;
                        const unsigned f = (((row >> 1) & 1) | (((row >> 3) & 1) << 1)) << 1;
                        x[hf] = lds_tr(kexp + row * 128 + (((2 * cs + (rp >> 1)) ^ f) * 16) + (rp & 1) * 8);
                    }
                    xq[tk * 4 + cs] = v4u{x[0].x, x[0].y, x[1].x, x[1].y};
                }
            SB();
            const unsigned vo = ((lane >> 3) & 1) * 32768 + (lane & 7) * 64 + g4 * 16;
#pragma unroll
            for (int i = 0; i < 8; ++i)
                __builtin_amdgcn_raw_buffer_store_b128(xq[i], ws, vo, WS_XT + (jj * 8 + 2 * (i & 3)) * 32768 + (tb * 8 + 2 * w + (i >> 2)) * 512, AUX_SC1);
            SB();
        }
        MEMFENCE();
        touch(lane * 1280, rpf, pfo + (w + 4) * 128);
        touch(lane * 1280, rpf, pfo + (w < 2 ? w + 8 : w + 4) * 128);
        STAMP(2);
        unsigned ga[32];
        {
            const unsigned tabm = stg + TAB_OFF - 2 * 0x4280;
#pragma unroll
            for (int rr = 0; rr < 4; ++rr)
#pragma unroll
                for (int e = 0; e < 4; ++e) {
                    const int l0 = 2044 + (lane & 3) - 32 * (16 * rr + (lane >> 2)) - 8 * e, l1 = l0 - 4;
                    const unsigned pk = pk2((float)l0, (float)l1);
                    unsigned g0 = tabm + 2 * (pk & 0xffffu), g1 = tabm + 2 * (pk >> 16);
                    if (rr == 3) {
                        if (l0 < 128) g0 = stg + TAB_OFF + 2 * l0;
                        if (l1 < 128) g1 = stg + TAB_OFF + 2 * l1;
                    }
                    ga[8 * rr + 2 * e] = g0; ga[8 * rr + 2 * e + 1] = g1;
                }
        }
        kg_hi(ks, stg, lane);
        kg_finish(stg, kexp, lane, ga);
        asm volatile("s_waitcnt vmcnt(2)\n\ts_barrier" ::: "memory");
        set_flag(ws, FL_XT + tb * 10 + jj, tA, tB);
        STAMP(3);
        ks = kg_lo(in[1], stg, lane);
        kg_hi(ks, stg, lane);
        SB();
        poll_flags(ws, WS_FLAG + (FL_XT + (lane & 7) * 10 + (grp >> 3)) * 256, tA, tB);
        v4u uu[8];
#pragma unroll
        for (int i = 0; i < 8; ++i)
            uu[i] = __builtin_amdgcn_raw_buffer_load_b128(ws, (lane >> 3) * 512 + (lane & 7) * 16, WS_XT + grp * 32768 + w * 128 + i * 4096, AUX_SC1);
        SB();
        kg_finish(stg, kexp + KEXP_CH, lane, ga);
        SB();
        {
            const unsigned ua = stg + ((lane >> 2) & 1) * U_CH + 64 + (lane >> 3) * 64 + (lane & 3) * 16;
#pragma unroll
            for (int i = 0; i < 8; ++i) lds_st<v4u>(ua + i * 512, uu[i]);
            if (lane < 8) lds_st<v4u>(stg + (lane >> 2) * U_CH + (lane & 3) * 16, v4u{0, 0, 0, 0});
        }
        STAMP(4);
        MEMFENCE();
        conv_channel<0>(kexp, stg, stg + YST_OFF, lane, cs, cf, stg + TAB_OFF);
        conv_channel<1>(kexp + KEXP_CH, stg + U_CH, stg + YST_OFF + 2, lane, cs, cf, stg + TAB_OFF);
        STAMP(5);
        {
            v4u yq[8];
#pragma unroll
            for (int i = 0; i < 8; ++i) yq[i] = lds_ld<v4u>(stg + YST_OFF + i * 1024 + lane * 16);
            SB();
#pragma unroll
            for (int i = 0; i < 8; ++i)
                __builtin_amdgcn_raw_buffer_store_b128(yq[i], ws, lane * 16, WS_Y + (grp * 4 + w) * 8192 + i * 1024, AUX_SC1);
        }
        asm volatile("s_waitcnt vmcnt(0) lgkmcnt(0)\n\ts_barrier" ::: "memory");
        set_flag(ws, FL_Y + grp, tA, tB);
        cbuf3 = 32768 + w * 10240; cbuf4 = 98304 + w * 10240;
        MEMFENCE();
#pragma unroll
        for (int i = 0; i < 10; ++i) c_dma(cs, 4, i, cbuf4);
    } else {
        const int gc = 2 * (2 * (xl & 3) + 1 - (xl >> 2)) + (r - 30);
        const v2u ep = __builtin_amdgcn_raw_buffer_load_b64(ws, WS_EPOCH, 0, AUX_SC1);
        const rsrc_t rgw = mk(p.gw, VBYTES);
        const unsigned vo = (lane >> 3) * 1280 + (lane & 7) * 16, so = (128 * gc + 32 * w) * 1280;
        float sum[4] = {0.f, 0.f, 0.f, 0.f};
        v4u gv[10], xv[10][4];
#pragma unroll
        for (int s = 0; s < 10; ++s) {
            gv[s] = __builtin_amdgcn_raw_buffer_load_b128(rgw, (lane & 7) * 16, s * 128, 0);
#pragma unroll
            for (int q = 0; q < 4; ++q) xv[s][q] = __builtin_amdgcn_raw_buffer_load_b128(rx, vo, so + q * 8 * 1280 + s * 128, 0);
        }
        SB();
#pragma unroll
        for (int s = 0; s < 10; ++s)
#pragma unroll
            for (int e = 0; e < 4; ++e) {
                const f32x2 gw2 = {bflo(gv[s][e]), bfhi(gv[s][e])};
#pragma unroll
                for (int q = 0; q < 4; ++q) {
                    const unsigned pr = pk2v(f32x2{bflo(xv[s][q][e]), bfhi(xv[s][q][e])} * gw2);
                    sum[q] += bflo(pr); sum[q] += bfhi(pr);
                }
            }
        tA = rfl(ep.x) + 1; tB = rfl(ep.y) + 1;
#pragma unroll
        for (int q = 0; q < 4; ++q) {
            float v = sum[q];
            v += __shfl_xor(v, 1); v += __shfl_xor(v, 2); v += __shfl_xor(v, 4);
            const float z = rbf(v), gt = rbf(1.f / (1.f + expf(-z)));
            if ((lane & 7) == 0)
                __builtin_amdgcn_raw_buffer_store_b32(__float_as_uint(gt), ws, WS_G + (128 * gc + 32 * w + 8 * q + (lane >> 3)) * 4, 0, AUX_SC1);
        }
        asm volatile("s_waitcnt vmcnt(0) lgkmcnt(0)\n\ts_barrier" ::: "memory");
        set_flag(ws, FL_G + gc, tA, tB);
        STAMP(5);
        MEMFENCE();
        cbuf3 = 32768 + w * 10240; cbuf4 = 98304 + w * 10240;
#pragma unroll
        for (int pb = 0; pb < 3; ++pb) {
#pragma unroll
            for (int i = 0; i < 10; ++i) c_dma(cs, pb, i, cbuf3);
            WAITV(0);
#pragma unroll
            for (int f = 0; f < 10; ++f) c_read(cs, cf, pb, f, cbuf3);
            asm volatile("s_waitcnt lgkmcnt(0)" ::: "memory");
        }
#pragma unroll
        for (int i = 0; i < 10; ++i) { c_dma(cs, 3, i, cbuf3); c_dma(cs, 4, i, cbuf4); }
    }

    const int own = (w + ct) & 3;
    const int tok = 64 * tt + 16 * own + (lane & 15), cq = lane >> 4;
    v2u xe[5], da[5], db[5];
    {
        const rsrc_t ra = mk(p.dska, VBYTES), rb = mk(p.dskb, VBYTES);
#pragma unroll
        for (int nt = 0; nt < 5; ++nt) {
            xe[nt] = __builtin_amdgcn_raw_buffer_load_b64(rx, tok * 1280 + cq * 8, ct * 160 + nt * 32, 0);
            da[nt] = __builtin_amdgcn_raw_buffer_load_b64(ra, cq * 8, ct * 160 + nt * 32, 0);
            db[nt] = __builtin_amdgcn_raw_buffer_load_b64(rb, cq * 8, ct * 160 + nt * 32, 0);
        }
    }
    {
        const int g0 = (10 * w) >> 2;
        poll_flags(ws, WS_FLAG + (lane < 3 ? FL_H + (tt >> 1) * 10 + g0 + lane : FL_G + (tt >> 1)) * 256, tA, tB);
    }
    STAMP(6);
    if (r < 20 || r >= 30) {
#pragma unroll
        for (int f = 0; f < 10; ++f) c_read(cs, cf, 3, f, cbuf3);
    }
#pragma unroll
    for (int f = 0; f < 10; ++f) c_read(cs, cf, 4, f, cbuf4);
    const float gate = __uint_as_float(__builtin_amdgcn_raw_buffer_load_b32(ws, WS_G + tok * 4, 0, AUX_SC1));
    f32x4 fin[5];
    {
        constexpr int WIN = 20;
        v4u hv[WIN];
        const unsigned hb = WS_H + (tt * 40 + 10 * w) * 4096;
        unsigned so[4];
#pragma unroll
        for (int o = 0; o < 4; ++o) so[o] = hb + ((own + 1 + o) & 3) * 1024;
#pragma unroll
        for (int i = 0; i < WIN; ++i) hv[i] = __builtin_amdgcn_raw_buffer_load_b128(ws, lane * 16, so[i / 10] + (i % 10) * 4096, AUX_SC1);
        SB();
#pragma unroll
        for (int o = 0; o < 4; ++o) {
            f32x4 acc[5];
#pragma unroll
            for (int j = 0; j < 10; ++j) {
                const int i = 10 * o + j;
                const v4u hf = hv[i % WIN];
#pragma unroll
                for (int nt = 0; nt < 5; ++nt) {
                    if (j == 0) MFMA16AZ(acc[nt], cf[nt], hf); else MFMA16A(acc[nt], cf[5 * j + nt], hf);
                }
                if (i + WIN < 40)
                    hv[i % WIN] = __builtin_amdgcn_raw_buffer_load_b128(ws, lane * 16, so[(i + WIN) / 10] + ((i + WIN) % 10) * 4096, AUX_SC1);
                SB();
            }
            asm volatile("s_nop 7" : "+v"(acc[0]), "+v"(acc[1]), "+v"(acc[2]), "+v"(acc[3]), "+v"(acc[4]));
            if (o < 3) {
                const unsigned ra = red_off(((own + 1 + o) & 3) * 3 + o) + lane * 16;
#pragma unroll
                for (int nt = 0; nt < 5; ++nt) lds_st<f32x4>(ra + nt * 1024, acc[nt]);
            } else {
#pragma unroll
                for (int nt = 0; nt < 5; ++nt) fin[nt] = acc[nt];
            }
            SB();
        }
    }
    STAMP(7);
    poll_flags(ws, WS_FLAG + (FL_Y + ct * 10 + (lane < 10 ? lane : 0)) * 256, tA, tB);
    unsigned yv[5][2];
#pragma unroll
    for (int nt = 0; nt < 5; ++nt)
#pragma unroll
        for (int e = 0; e < 2; ++e)
            yv[nt][e] = __builtin_amdgcn_raw_buffer_load_b32(ws, tok * 4 + cq * 16384, WS_Y + (40 * ct + 8 * nt + e) * 8192, AUX_SC1);
    BARRIER();
#pragma unroll
    for (int o = 0; o < 3; ++o)
#pragma unroll
        for (int nt = 0; nt < 5; ++nt) fin[nt] += lds_ld<f32x4>(red_off(own * 3 + o) + lane * 16 + nt * 1024);
    STAMP(8);
    {
        const rsrc_t ro = mk(p.out, SEQ * DM * 4);
        const float omg = rbf(1.f - gate);
        f32x4 oo[5];
#pragma unroll
        for (int nt = 0; nt < 5; ++nt) {
            f32x4 o;
#pragma unroll
            for (int e = 0; e < 2; ++e) {
                const f32x2 x2 = {bflo(xe[nt][e]), bfhi(xe[nt][e])};
                const unsigned sa = pk2v(x2 * f32x2{bflo(da[nt][e]), bfhi(da[nt][e])});
                const unsigned sb = pk2v(x2 * f32x2{bflo(db[nt][e]), bfhi(db[nt][e])});
                const unsigned bb = pk2v(f32x2{bflo(yv[nt][e]), bfhi(yv[nt][e])} + f32x2{bflo(sb), bfhi(sb)});
                const unsigned ob = pk2v(f32x2{bflo(bb), bfhi(bb)} * omg);
                const float a0 = fin[nt][2 * e] + bflo(sa), a1 = fin[nt][2 * e + 1] + bfhi(sa);
                o[2 * e] = __fadd_rn(__fmul_rn(gate, a0), bflo(ob));
                o[2 * e + 1] = __fadd_rn(__fmul_rn(gate, a1), bfhi(ob));
            }
            oo[nt] = o;
        }
        SB();
#pragma unroll
        for (int nt = 0; nt < 5; ++nt)
            __builtin_amdgcn_raw_buffer_store_b128(__builtin_bit_cast(v4u, oo[nt]), ro, tok * 2560 + cq * 16, ct * 320 + nt * 64, 0);
        asm volatile("s_nop 1");
        SB();
    }
    if (b == 255 && w == 0) {
        for (int k = 0; k < 4; ++k) {
            const int i = k * 64 + lane;
            poll_flags(ws, WS_FLAG + (i < 240 ? i : i + 80) * 256, tA, tB);
        }
        if (lane == 0) __builtin_amdgcn_raw_buffer_store_b64(v2u{tA, tB}, ws, WS_EPOCH, 0, AUX_SC1);
    }
    asm volatile("s_waitcnt vmcnt(0)" ::: "memory");
    STAMP(9);
    if (tid < 16) ((GAS v4u*)((char*)p.ws + WS_STAMP))[blockIdx.x * 16 + tid] = lds_ld<v4u>(163328 + tid * 16);
    if (b == RWG && w == 0) {
        const v4u s0 = lds_ld<v4u>(163328 + RFROM * 16), s1 = lds_ld<v4u>(163328 + RTO * 16);
        unsigned d = RCYC ? (rfl(s1.z) - rfl(s0.z)) / 10 : (rfl(s1.x) - rfl(s0.x)) * 10;
        if (d > 100000u) d = 100000u;
        const u64 t0 = __builtin_amdgcn_s_memrealtime();
        while (__builtin_amdgcn_s_memrealtime() - t0 < d) __builtin_amdgcn_s_sleep(2);
    }
}

extern "C" void kernel_launch(void* const* d_in, const int* in_sizes, int n_in, void* d_out, int out_size, void* d_ws,
                              size_t ws_size, hipStream_t stream) {
    Params p;
    p.x = d_in[0]; p.lre = d_in[1]; p.lim = d_in[2]; p.bre = d_in[3]; p.bim = d_in[4]; p.cre = d_in[5]; p.cim = d_in[6];
    p.ldt = d_in[7]; p.dska = d_in[8];
    p.sare = d_in[10]; p.saim = d_in[11]; p.sbre = d_in[12]; p.sbim = d_in[13]; p.scre = d_in[14]; p.scim = d_in[15];
    p.sldt = d_in[16]; p.dskb = d_in[17]; p.gw = d_in[18];
    p.out = d_out; p.ws = d_ws;
    (void)hipFuncSetAttribute((const void*)k_fused, hipFuncAttributeMaxDynamicSharedMemorySize, LDS_BYTES);
    k_fused<<<256, 256, LDS_BYTES, stream>>>(p);
}
```

```cpp
#include <hip/hip_runtime.h>
#include <stdint.h>

#define DIAG 0

#define DI __device__ __forceinline__
#define GAS __attribute__((address_space(1)))
#define LAS __attribute__((address_space(3)))
typedef unsigned short u16;
typedef unsigned long long u64;
typedef unsigned v4u __attribute__((ext_vector_type(4)));
typedef unsigned v2u __attribute__((ext_vector_type(2)));
typedef float f32x2 __attribute__((ext_vector_type(2)));
typedef float f32x4 __attribute__((ext_vector_type(4)));
typedef float f32x16 __attribute__((ext_vector_type(16)));
typedef short s16x4 __attribute__((ext_vector_type(4)));
typedef __bf16 bf16x2 __attribute__((ext_vector_type(2)));
typedef __amdgpu_buffer_rsrc_t rsrc_t;

constexpr int SEQ = 2048, DM = 640, NMODE = 128;
constexpr unsigned XBYTES = SEQ * DM * 2, MBYTES = DM * DM * 2, SBYTES = DM * NMODE * 2, VBYTES = DM * 2;

constexpr unsigned WS_EPOCH = 0;
constexpr unsigned WS_FLAG = 4096;
constexpr unsigned FL_H = 0, FL_XT = 160, FL_Y = 240, FL_G = 320, FL_N = 336;
constexpr unsigned WS_G = 96 * 1024;
constexpr unsigned WS_AGG = 128 * 1024;
constexpr unsigned WS_H = 512 * 1024;
constexpr unsigned WS_XT = WS_H + SEQ * 1280 * 2;
constexpr unsigned WS_Y = WS_XT + XBYTES;
constexpr unsigned WS_END = WS_Y + XBYTES;
constexpr unsigned WS_STAMP = 16u << 20;

constexpr int LDS_BYTES = 163840;
constexpr unsigned LDS_DUMMY = LDS_BYTES - 256;
constexpr int SPIN_CAP = 20000;
constexpr int AUX_SC1 = 16;

#define NC1 20
#define NC2 10

struct Params {
    const void *x, *lre, *lim, *bre, *bim, *cre, *cim, *ldt, *dska;
    const void *sare, *saim, *sbre, *sbim, *scre, *scim, *sldt, *dskb, *gw;
    void *out, *ws;
};

DI rsrc_t mk(const void* p, unsigned bytes) { return __builtin_amdgcn_make_buffer_rsrc((void*)p, (short)0, (int)bytes, 0x00020000); }
DI float bflo(unsigned v) { return __uint_as_float(v << 16); }
DI float bfhi(unsigned v) { return __uint_as_float(v & 0xffff0000u); }
DI unsigned pk2(float a, float b) { f32x2 v = {a, b}; return __builtin_bit_cast(unsigned, __builtin_convertvector(v, bf16x2)); }
DI unsigned pk2v(f32x2 v) { return __builtin_bit_cast(unsigned, __builtin_convertvector(v, bf16x2)); }
DI float rbf(float a) { return bflo(pk2(a, a)); }
DI unsigned rfl(unsigned v) { return __builtin_amdgcn_readfirstlane(v); }
#define SB() __builtin_amdgcn_sched_barrier(0)
#define MEMFENCE() asm volatile("" ::: "memory")

template <class T> DI T lds_ld(unsigned a) { return *(const LAS T*)(uintptr_t)a; }
template <class T> DI void lds_st(unsigned a, T v) { *(LAS T*)(uintptr_t)a = v; }
DI v2u lds_tr(unsigned a) { return __builtin_bit_cast(v2u, __builtin_amdgcn_ds_read_tr16_b64_v4i16((LAS s16x4*)(uintptr_t)a)); }

#define MFMA32(acc, a, b) asm volatile("v_mfma_f32_32x32x16_bf16 %0, %1, %2, %0" : "+v"(acc) : "v"(a), "v"(b))
#define MFMA32Z(acc, a, b) asm volatile("v_mfma_f32_32x32x16_bf16 %0, %1, %2, 0" : "=&v"(acc) : "v"(a), "v"(b))
#define MFMA16A(acc, a, b) asm volatile("v_mfma_f32_16x16x32_bf16 %0, %1, %2, %0" : "+v"(acc) : "a"(a), "v"(b))
#define MFMA16AZ(acc, a, b) asm volatile("v_mfma_f32_16x16x32_bf16 %0, %1, %2, 0" : "=&v"(acc) : "a"(a), "v"(b))

DI void dma16(unsigned ldsoff, unsigned voff, rsrc_t rs, unsigned soff) {
    asm volatile("s_mov_b32 m0, %0\n\ts_nop 0\n\tbuffer_load_dwordx4 %1, %2, %3 offen lds" ::"s"(ldsoff), "v"(voff), "s"(rs), "s"(soff) : "memory");
}
DI void touch(unsigned voff, rsrc_t rs, unsigned soff) {
    asm volatile("s_mov_b32 m0, %0\n\ts_nop 0\n\tbuffer_load_dword %1, %2, %3 offen lds" ::"s"(LDS_DUMMY), "v"(voff), "s"(rs), "s"(soff) : "memory");
}
#define WAITV(n) asm volatile("s_waitcnt vmcnt(%0)" ::"n"(n) : "memory")
#define BARRIER() asm volatile("s_waitcnt lgkmcnt(0)\n\ts_barrier" ::: "memory")

#define STAMP(i) do {} while (0)

DI void poll_flags(rsrc_t ws, unsigned off, unsigned tA, unsigned tB) {
    for (int spin = 0; spin < SPIN_CAP; ++spin) {
        MEMFENCE();
        const v2u f = __builtin_amdgcn_raw_buffer_load_b64(ws, off, 0, AUX_SC1);
        if (__all((f.x == tA) & (f.y == tB))) break;
    }
    __builtin_amdgcn_fence(__ATOMIC_ACQUIRE, "wavefront");
    MEMFENCE();
}
DI void set_flag(rsrc_t ws, unsigned idx, unsigned tA, unsigned tB) {
    if (threadIdx.x == 0) __builtin_amdgcn_raw_buffer_store_b64(v2u{tA, tB}, ws, WS_FLAG + idx * 256, 0, AUX_SC1);
}

struct cpx { float r, i; };
DI cpx cmul(cpx a, cpx b) { return {a.r * b.r - a.i * b.i, a.r * b.i + a.i * b.r}; }
DI cpx cfma(cpx a, cpx b, cpx c) { return {a.r * b.r - a.i * b.i + c.r, a.r * b.i + a.i * b.r + c.i}; }
struct cp2 { f32x2 r, i; };
DI cp2 cmul2(cp2 a, cp2 b) { return {a.r * b.r - a.i * b.i, a.r * b.i + a.i * b.r}; }
DI cp2 cmul2s(cp2 a, cpx b) { return {a.r * b.r - a.i * b.i, a.r * b.i + a.i * b.r}; }
DI void halves(float v, float& lo, float& hi) {
    auto s = __builtin_amdgcn_permlane32_swap(__float_as_uint(v), __float_as_uint(v), false, false);
    lo = __uint_as_float(s[0]); hi = __uint_as_float(s[1]);
}

struct CStage { rsrc_t r0, r1; unsigned vo[2], ra[2]; int w; };
DI void c_dma(const CStage& c, int pb, int i, unsigned buf) {
    const unsigned u = 5u * c.w + pb;
    dma16(buf + i * 1024, c.vo[i & 1], (pb & 1) ? c.r1 : c.r0, (u >> 1) * 128u + i * 8u * 1280u);
}
DI void c_read(const CStage& c, v4u (&cf)[50], int pb, int f, unsigned buf) {
    cf[10 * pb + f] = lds_ld<v4u>(buf + c.ra[f / 5] + (f % 5) * 2048);
}

struct KgIn { unsigned are, aim, bre, bim, cre, cim, ldt; };
DI cp2 cexp2(f32x2 zr2, f32x2 zi2, float l) {
    const f32x2 a = zr2 * l, b = zi2 * l;
    cp2 o;
    const float m0 = __builtin_amdgcn_exp2f(a.x), m1 = __builtin_amdgcn_exp2f(a.y);
    const float f0 = __builtin_amdgcn_fractf(b.x), f1 = __builtin_amdgcn_fractf(b.y);
    o.r = f32x2{m0 * __builtin_amdgcn_cosf(f0), m1 * __builtin_amdgcn_cosf(f1)};
    o.i = f32x2{m0 * __builtin_amdgcn_sinf(f0), m1 * __builtin_amdgcn_sinf(f1)};
    return o;
}
constexpr unsigned STG_ROW = 528, STG_BYTES = 53 * STG_ROW, KEXP_BASE = 4 * STG_BYTES, KEXP_RHO = 1216, KEXP_CH = 4 * KEXP_RHO;
constexpr unsigned U_CH = 4160, YST_OFF = 2 * U_CH, TAB_OFF = YST_OFF + 8192;
DI void put_lo(unsigned a, int row, cp2 v) { lds_st<v2u>(a + (21 + row) * STG_ROW, v2u{pk2v(v.r), pk2v(v.i)}); }
DI void put_hi(unsigned a, int row, cp2 v) { lds_st<v2u>(a + row * STG_ROW, v2u{pk2v(v.r), pk2v(-v.i)}); }

struct KgState { f32x2 zr2, zi2; cp2 w; };
DI KgState kg_lo(const KgIn& in, unsigned stg, int lane) {
    const float dt = rbf(__expf(bflo(in.ldt)));
    KgState s;
    const f32x2 zr = f32x2{bflo(in.are), bfhi(in.are)} * dt, zi = f32x2{bflo(in.aim), bfhi(in.aim)} * dt;
    s.zr2 = zr * 1.44269504088896341f;
    s.zi2 = zi * 0.15915494309189535f;
    const cp2 db = {f32x2{bflo(in.bre), bfhi(in.bre)} * dt, f32x2{bflo(in.bim), bfhi(in.bim)} * dt};
    const cp2 cc = {f32x2{bflo(in.cre), bfhi(in.cre)}, f32x2{bflo(in.cim), bfhi(in.cim)}};
    s.w = cmul2(cc, db);
    const unsigned a = stg + lane * 8;
    const cp2 e8 = cexp2(s.zr2, s.zi2, 8.f), e32 = cexp2(s.zr2, s.zi2, 32.f);
    cp2 v0 = {f32x2{1.f, 1.f}, f32x2{0.f, 0.f}}, v1 = e8, v2 = cmul2(e8, e8), v3 = cmul2(v2, e8);
    put_lo(a, 0, v0); put_lo(a, 1, v1); put_lo(a, 2, v2); put_lo(a, 3, v3);
    v0 = e32;
#pragma unroll
    for (int k = 1; k < 8; ++k) {
        if (k > 1) v0 = cmul2(v0, e32);
        v1 = cmul2(v1, e32); v2 = cmul2(v2, e32); v3 = cmul2(v3, e32);
        put_lo(a, 4 * k, v0); put_lo(a, 4 * k + 1, v1); put_lo(a, 4 * k + 2, v2); put_lo(a, 4 * k + 3, v3);
    }
    return s;
}
DI void kg_hi(const KgState& s, unsigned stg, int lane) {
    const unsigned a = stg + lane * 8;
    const cp2 e1 = cexp2(s.zr2, s.zi2, 1.f), e256 = cexp2(s.zr2, s.zi2, 256.f);
    const cp2 e2 = cmul2(e1, e1), e4 = cmul2(e2, e2);
    cp2 w0 = s.w, w1 = cmul2(w0, e1), w2 = cmul2(w0, e2), w3 = cmul2(w1, e2);
    put_hi(a, 0, w0); put_hi(a, 1, w1); put_hi(a, 2, w2); put_hi(a, 3, w3);
    put_hi(a, 4, cmul2(w0, e4)); put_hi(a, 5, cmul2(w1, e4)); put_hi(a, 6, cmul2(w2, e4)); put_hi(a, 7, cmul2(w3, e4));
    cp2 q = cmul2(w0, e256);
    const cp2 q2 = cmul2(q, e2);
    put_hi(a, 8, q); put_hi(a, 9, q2); put_hi(a, 10, cmul2(q, e4)); put_hi(a, 11, cmul2(q2, e4));
    q = cmul2(q, e256); put_hi(a, 12, q); put_hi(a, 13, cmul2(q, e4));
    q = cmul2(q, e256); put_hi(a, 14, q); put_hi(a, 15, cmul2(q, e4));
#pragma unroll
    for (int m = 0; m < 5; ++m) { q = cmul2(q, e256); put_hi(a, 16 + m, q); }
}
DI void kg_finish(unsigned stg, unsigned kexp, int lane, const unsigned (&ga)[32]) {
    const int j = lane & 31, hh = lane >> 5;
    const unsigned ra = stg + j * STG_ROW + hh * 16;
    f32x16 d;
#pragma unroll
    for (int ks = 0; ks < 16; ++ks) {
        const v4u a = lds_ld<v4u>(ra + ks * 32), b = lds_ld<v4u>(ra + 21 * STG_ROW + ks * 32);
        if (ks == 0) MFMA32Z(d, a, b); else MFMA32(d, a, b);
    }
    asm volatile("s_nop 11" : "+v"(d));
    const unsigned tab = stg + TAB_OFF;
    lds_st<v2u>(tab + (8 * j + 4 * hh) * 2, v2u{pk2(d[0], d[1]), pk2(d[2], d[3])});
    const unsigned a1 = tab + (hh ? 384 + 2 * j : 256 + 4 * j) * 2, a2 = tab + (hh ? 448 + 2 * j : 258 + 4 * j) * 2;
    lds_st<unsigned>(a1, pk2(d[4], d[5]));
    lds_st<unsigned>(a2, pk2(d[6], d[7]));
    const unsigned a3 = tab + (hh ? 640 + j : 512 + j) * 2;
    const unsigned p89 = pk2(d[8], d[9]), pab = pk2(d[10], d[11]);
    lds_st<u16>(a3, (u16)p89); lds_st<u16>(a3 + 64, (u16)(p89 >> 16));
    lds_st<u16>(a3 + 128, (u16)pab); lds_st<u16>(a3 + 192, (u16)(pab >> 16));
    const unsigned wa = kexp + (lane & 3) * KEXP_RHO + (lane >> 2) * 16;
#pragma unroll
    for (int r = 0; r < 4; ++r) {
        v4u o;
#pragma unroll
        for (int e = 0; e < 4; ++e) {
            const unsigned lo = lds_ld<u16>(ga[8 * r + 2 * e]), hi = lds_ld<u16>(ga[8 * r + 2 * e + 1]);
            o[e] = lo | (hi << 16);
        }
        lds_st<v4u>(wa + r * 256, o);
    }
    if (lane < 28) lds_st<v4u>(wa + 1024, v4u{0, 0, 0, 0});
}

#define SHADD(d, prev, cur, y) asm volatile("v_add_f32_dpp %0, %1, %3 wave_ror:1 row_mask:0xf bank_mask:0xf\n\t" \
    "v_add_f32_dpp %0, %2, %3 wave_shr:1 row_mask:0xf bank_mask:0xf" : "=&v"(d) : "v"(prev), "v"(cur), "v"(y))
#define SHADD0(d, cur, y) asm volatile("v_add_f32_dpp %0, %1, %2 wave_shr:1 row_mask:0xf bank_mask:0xf bound_ctrl:0" : "=&v"(d) : "v"(cur), "v"(y))

template <int E>
DI void conv_channel(unsigned kexp, unsigned ub, unsigned yst, int lane, const CStage& cs, v4u (&cf)[50], unsigned cbuf) {
    const int hh = lane >> 5;
    const unsigned aA = kexp + (lane & 3) * KEXP_RHO + (56 - 8 * ((lane & 31) >> 2) + 8 * hh) * 2;
    const unsigned aB = ub + 2 * (4 + 32 * hh + 4 * ((lane >> 2) & 3) + 16 * ((lane >> 4) & 1) + 4 * (lane & 3));
    f32x16 acc[8];
    v4u af[2][8];
#pragma unroll
    for (int dl = 0; dl < 8; ++dl) af[0][dl] = lds_ld<v4u>(aA + (64 * (7 - dl)) * 2);
    v2u b0 = lds_tr(aB), b1 = lds_tr(aB + 32);
#pragma unroll
    for (int c = 0; c < 4; ++c) {
#pragma unroll
        for (int i = 0; i < 8; ++i) {
            const v4u bf = {b0.x, b0.y, b1.x, b1.y};
            const int ni = (i + 1) & 7, nc = c + (i == 7);
            if (nc < 4) { b0 = lds_tr(aB + 128 * (4 * ni + nc)); b1 = lds_tr(aB + 128 * (4 * ni + nc) + 32); }
            if (c < 3) af[(c + 1) & 1][i] = lds_ld<v4u>(aA + (64 * (7 - i) + 16 * (c + 1)) * 2);
            SB();
#pragma unroll
            for (int dl = 0; dl < 8 - i; ++dl) {
                if (c == 0 && i == 0) MFMA32Z(acc[dl], af[0][dl], bf);
                else MFMA32(acc[i + dl], af[c & 1][dl], bf);
                const int m = 36 * c + 8 * i - i * (i - 1) / 2 + dl, pb = 2 * E + m / 72, mm = m % 72;
                if (mm < 40 && (mm & 3) == 0) { SB(); c_dma(cs, pb, mm >> 2, cbuf); SB(); }
                if (mm == 59) { SB(); WAITV(0); SB(); }
                if (mm >= 60 && mm < 70) { SB(); c_read(cs, cf, pb, mm - 60, cbuf); SB(); }
                if (mm == 71) { SB(); asm volatile("s_waitcnt lgkmcnt(0)" ::: "memory"); SB(); }
            }
            SB();
        }
    }
    asm volatile("s_nop 11" : "+v"(acc[7]));
    float p2[32], p1[32], y[32];
#pragma unroll
    for (int G = 0; G < 32; ++G) {
        const float cur = acc[G >> 2][4 * (G & 3) + 3], yy = acc[G >> 2][4 * (G & 3) + 2];
        if (G == 0) SHADD0(p2[0], cur, yy);
        else { const float prev = acc[(G - 1) >> 2][4 * ((G - 1) & 3) + 3]; SHADD(p2[G], prev, cur, yy); }
    }
#pragma unroll
    for (int G = 0; G < 32; ++G) {
        const float yy = acc[G >> 2][4 * (G & 3) + 1];
        if (G == 0) SHADD0(p1[0], p2[0], yy); else SHADD(p1[G], p2[G - 1], p2[G], yy);
    }
#pragma unroll
    for (int G = 0; G < 32; ++G) {
        const float yy = acc[G >> 2][4 * (G & 3)];
        if (G == 0) SHADD0(y[0], p1[0], yy); else SHADD(y[G], p1[G - 1], p1[G], yy);
    }
    const unsigned ya = yst + lane * 4;
#pragma unroll
    for (int G = 0; G < 32; G += 2) {
        const unsigned pk = pk2(y[G], y[G + 1]);
        lds_st<u16>(ya + G * 256, (u16)pk);
        lds_st<u16>(ya + G * 256 + 256, (u16)(pk >> 16));
    }
}

DI unsigned red_off(int i) { return i * 5120 + (i >= 6 ? 43008 : 0) + (i >= 10 ? 45056 : 0); }

__global__ __launch_bounds__(256, 1) void k_fused(Params p) {
    const int tid = threadIdx.x, lane = tid & 63;
    const int w = __builtin_amdgcn_readfirstlane(tid >> 6);
    const int b = blockIdx.x, xl = b & 7, r = b >> 3;
    STAMP(0);
    const rsrc_t ws = mk(p.ws, WS_END);
    const rsrc_t rx = mk(p.x, XBYTES);
    const int rn = r < 20 ? r : r - 20;
    const int tti = rn & 3, kk5 = rn >> 2;
    const int ct = (2 * tti + (r < 20 ? (kk5 < 2 ? kk5 + 2 : kk5 + 3) : (kk5 < 2 ? kk5 : 4))) & 7;
    const int tt = 4 * xl + tti;
    CStage cs;
    cs.r0 = mk((w & 1) ? p.cim : p.cre, MBYTES); cs.r1 = mk((w & 1) ? p.cre : p.cim, MBYTES); cs.w = w;
#pragma unroll
    for (int e = 0; e < 2; ++e) {
        cs.vo[e] = (80 * ct + (lane >> 3)) * 1280 + (((lane & 7) ^ ((4 * e + (lane >> 4)) & 7)) * 16);
        cs.ra[e] = (lane & 15) * 128 + (((4 * e + (lane >> 4)) ^ ((lane >> 1) & 7)) * 16);
    }
    v4u cf[50];
    unsigned tA, tB, cbuf3, cbuf4;

    if (r < 20) {
        const int c = 4 * (xl & 3) + (r & 3), g = 5 * (xl >> 2) + (r >> 2);
        const int th = w & 1, cg = w >> 1, ch = lane & 31, hh = lane >> 5;
        const rsrc_t rbre = mk(p.bre, MBYTES), rbim = mk(p.bim, MBYTES);
        const int pch = 64 * g + 32 * cg + ch;
        unsigned vlr, vli, vld; v2u ep;
        {
            const rsrc_t r0 = mk(p.lre, VBYTES), r1 = mk(p.lim, VBYTES), r2 = mk(p.ldt, VBYTES);
            asm volatile("s_nop 4\n\tbuffer_load_ushort %0, %4, %5, 0 offen\n\tbuffer_load_ushort %1, %4, %6, 0 offen\n\t"
                         "buffer_load_ushort %2, %4, %7, 0 offen\n\tbuffer_load_dwordx2 %3, off, %8, 0 sc1"
                         : "=&v"(vlr), "=&v"(vli), "=&v"(vld), "=&v"(ep) : "v"(pch * 2), "s"(r0), "s"(r1), "s"(r2), "s"(ws) : "memory");
        }
        const int rb = lane >> 3;
        const unsigned gch = ((lane & 7) ^ ((4 * w + (rb >> 1)) & 7)) * 16;
        const unsigned vox = (32 * (rb >> 2) + (rb & 3)) * 1280 + gch, vob = rb * 1280 + gch;
        const unsigned sox = (128 * c + 4 * w) * 1280, sob = (64 * g + 8 * w) * 1280;
        auto dma_piece = [&](int ti, unsigned slot, unsigned ko) {
            const unsigned l = slot + ti * 4096 + w * 1024;
            if (ti < 4) dma16(l, vox, rx, sox + (64 * (ti >> 1) + 16 * (ti & 1)) * 1280 + ko);
            else dma16(l, vob, (ti & 1) ? rbim : rbre, sob + 32 * ((ti - 4) >> 1) * 1280 + ko);
        };
#pragma unroll
        for (int ti = 0; ti < 8; ++ti) dma_piece(ti, 0, 0);
        asm volatile("s_waitcnt vmcnt(8)" : "+v"(vlr), "+v"(vli), "+v"(vld), "+v"(ep)::"memory");
        tA = rfl(ep.x) + 1; tB = rfl(ep.y) + 1;
        cpx lam, S2;
        {
            const float dt = rbf(__expf(bflo(vld))), Lr = bflo(vlr), Li = bflo(vli);
            const float e = __expf(dt * Lr), fr = __builtin_amdgcn_fractf((dt * Li) * 0.15915494309189535f);
            lam = {e * __builtin_amdgcn_cosf(fr), e * __builtin_amdgcn_sinf(fr)};
            const float nr = lam.r - 1.f, ni = lam.i, den = 2.f * __builtin_amdgcn_rcpf(Lr * Lr + Li * Li);
            S2 = {(nr * Lr + ni * Li) * den, (ni * Lr - nr * Li) * den};
        }
        cp2 Q[16];
        Q[0] = {f32x2{1.f, lam.r}, f32x2{0.f, lam.i}};
        const cpx l2 = cmul(lam, lam), l4 = cmul(l2, l2), l8 = cmul(l4, l4), l16 = cmul(l8, l8), l32 = cmul(l16, l16);
        SB(); dma_piece(0, 32768, 128); dma_piece(1, 32768, 128); SB();
        Q[1] = cmul2s(Q[0], l2);
#pragma unroll
        for (int i = 0; i < 2; ++i) Q[2 + i] = cmul2s(Q[i], l4);
        SB(); dma_piece(2, 32768, 128); SB();
#pragma unroll
        for (int i = 0; i < 4; ++i) Q[4 + i] = cmul2s(Q[i], l8);
        SB(); dma_piece(3, 32768, 128); SB();
#pragma unroll
        for (int i = 0; i < 8; ++i) { Q[8 + i] = cmul2s(Q[i], l16); if ((i & 1) == 1) { SB(); dma_piece(4 + (i >> 1), 32768, 128); SB(); } }
        const cpx l64 = cmul(l32, l32), l128 = cmul(l64, l64), l256 = cmul(l128, l128), l512 = cmul(l256, l256);
        const int seg = 2 * th + hh;
        cpx lseg = {1.f, 0.f}, lgat = {1.f, 0.f};
        if (hh) { lseg = l32; lgat = l128; }
        if (th) { lseg = cmul(lseg, l64); lgat = cmul(lgat, l256); }
        const cpx Sl = cmul(S2, lam);

        f32x16 aR[2], aI[2];
#pragma unroll
        for (int i = 0; i < 16; ++i) { aR[0][i] = 0.f; aR[1][i] = 0.f; aI[0][i] = 0.f; aI[1][i] = 0.f; }
        unsigned adA[4], adB[4];
        {
            const unsigned lb = ch * 128 + ((hh ^ ((ch >> 1) & 7)) * 16);
#pragma unroll
            for (int k = 0; k < 4; ++k) { adA[k] = (lb ^ (k * 32)) + th * 8192; adB[k] = (lb ^ (k * 32)) + 16384 + cg * 8192; }
        }
        STAMP(1);
        asm volatile("s_waitcnt vmcnt(8)\n\ts_barrier" : "+v"(aR[0]), "+v"(aI[0]), "+v"(aR[1]), "+v"(aI[1])::"memory");
        v4u fa0 = lds_ld<v4u>(adA[0]), fa1 = lds_ld<v4u>(adA[0] + 4096), fr = lds_ld<v4u>(adB[0]), fi = lds_ld<v4u>(adB[0] + 4096);
        const unsigned cb0 = 98304 + w * 10240, cb1 = 32768 + w * 10240;
#pragma unroll
        for (int s = 0; s < 10; ++s) {
#pragma unroll
            for (int k = 0; k < 4; ++k) {
                v4u na0 = fa0, na1 = fa1, nr = fr, ni = fi;
                if (k == 3) {
                    if (s <= 8) asm volatile("s_waitcnt vmcnt(6) lgkmcnt(0)\n\ts_barrier" ::: "memory");
                }
                const int kn = (k + 1) & 3;
                const unsigned so = ((k == 3 ? s + 1 : s) % 3) * 32768;
                const bool rd = !(k == 3 && s == 9);
                MFMA32(aR[0], fa0, fr); SB();
                if (rd) { na0 = lds_ld<v4u>(adA[kn] + so); nr = lds_ld<v4u>(adB[kn] + so); } SB();
                MFMA32(aI[0], fa0, fi); SB();
                if (rd) { ni = lds_ld<v4u>(adB[kn] + so + 4096); na1 = lds_ld<v4u>(adA[kn] + so + 4096); } SB();
                MFMA32(aR[1], fa1, fr);
                if (s <= 7) dma_piece(2 * k, ((s + 2) % 3) * 32768, (s + 2) * 128);
                else if (s == 8) c_dma(cs, 0, 2 * k, cb0);
                else if (k == 0) c_dma(cs, 0, 8, cb0);
                MFMA32(aI[1], fa1, fi);
                if (s <= 7) dma_piece(2 * k + 1, ((s + 2) % 3) * 32768, (s + 2) * 128);
                else if (s == 8) c_dma(cs, 0, 2 * k + 1, cb0);
                else if (k == 0) c_dma(cs, 0, 9, cb0);
                SB();
                fa0 = na0; fa1 = na1; fr = nr; fi = ni;
            }
        }
        asm volatile("s_nop 11" : "+v"(aR[0]), "+v"(aI[0]), "+v"(aR[1]), "+v"(aI[1]));
        STAMP(2);
        cpx agg;
        {
            f32x2 sr0 = {0.f, 0.f}, si0 = {0.f, 0.f}, sr1 = {0.f, 0.f}, si1 = {0.f, 0.f};
#pragma unroll
            for (int i = 0; i < 8; ++i) {
                const f32x2 ur = {aR[0][2 * i], aR[0][2 * i + 1]}, ui = {aI[0][2 * i], aI[0][2 * i + 1]};
                const f32x2 qr = Q[15 - i].r.yx, qi = Q[15 - i].i.yx;
                sr0 += qr * ur; si0 += qr * ui; sr0 -= qi * ui; si0 += qi * ur;
                const f32x2 vr = {aR[1][2 * i], aR[1][2 * i + 1]}, vi = {aI[1][2 * i], aI[1][2 * i + 1]};
                const f32x2 pr = Q[7 - i].r.yx, pi = Q[7 - i].i.yx;
                sr1 += pr * vr; si1 += pr * vi; sr1 -= pi * vi; si1 += pi * vr;
            }
            sr0 += sr1; si0 += si1;
            agg = {sr0.x + sr0.y, si0.x + si0.y};
        }
        cpx a0, a1;
        halves(agg.r, a0.r, a1.r); halves(agg.i, a0.i, a1.i);
        const cpx a64 = cfma(l32, a0, a1);
        const unsigned xch = 159744 + (cg * 2) * 256 + ch * 8;
        if (hh == 0) lds_st<f32x2>(xch + th * 256, f32x2{a64.r, a64.i});
        BARRIER();
        cpx pre = {0.f, 0.f};
        if (th) {
            const f32x2 o = lds_ld<f32x2>(xch);
            const cpx o64 = {o.x, o.y};
            const cpx a128 = cfma(l64, o64, a64);
            if (hh == 0 && c < 15)
                __builtin_amdgcn_raw_buffer_store_b128(v4u{__float_as_uint(a128.r), tA, __float_as_uint(a128.i), tB}, ws,
                                                       WS_AGG + (c * 640 + pch) * 16, 0, AUX_SC1);
            pre = hh ? cfma(l32, o64, a0) : o64;
        } else if (hh) pre = a0;
        STAMP(3);
        {
            cpx v = {0.f, 0.f};
#pragma unroll
            for (int t = 0; t < 32; ++t) {
                v = cfma(lam, v, cpx{aR[t >> 4][t & 15], aI[t >> 4][t & 15]});
                const cpx o = cmul(S2, v);
                aR[t >> 4][t & 15] = o.r; aI[t >> 4][t & 15] = o.i;
                SB();
                if (t < 10) c_dma(cs, 1, t, cb1);
                if (t == 10) { if (th && c < 15) WAITV(11); else WAITV(10); }
                if (t >= 10 && t < 20) c_read(cs, cf, 0, t - 10, cb0);
                if (t == 21) asm volatile("s_waitcnt lgkmcnt(0)" ::: "memory");
                if (t >= 21 && t < 31) c_dma(cs, 2, t - 21, cb0);
                SB();
            }
        }
        asm volatile("" : "+v"(aR[0]), "+v"(aI[0]), "+v"(aR[1]), "+v"(aI[1]));
        cpx carry = {0.f, 0.f};
        if (c > 0) {
            v4u ga[4];
            for (int spin = 0; spin < SPIN_CAP; ++spin) {
                MEMFENCE();
                bool ok = true;
#pragma unroll
                for (int i = 0; i < 4; ++i) {
                    const int j = c - 1 - (4 * i + seg);
                    ga[i] = __builtin_amdgcn_raw_buffer_load_b128(ws, WS_AGG + ((j < 0 ? 0 : j) * 640 + pch) * 16, 0, AUX_SC1);
                }
#pragma unroll
                for (int i = 0; i < 4; ++i) ok = ok & ((c - 1 - (4 * i + seg) < 0) | ((ga[i].y == tA) & (ga[i].w == tB)));
                if (__all(ok)) break;
            }
            cpx part = {0.f, 0.f};
#pragma unroll
            for (int i = 3; i >= 0; --i) {
                const bool val = c - 1 - (4 * i + seg) >= 0;
                const cpx a = {val ? __uint_as_float(ga[i].x) : 0.f, val ? __uint_as_float(ga[i].z) : 0.f};
                part = cfma(l512, part, a);
            }
            part = cmul(lgat, part);
            cpx p0, p1;
            halves(part.r, p0.r, p1.r); halves(part.i, p0.i, p1.i);
            const cpx ps = {p0.r + p1.r, p0.i + p1.i};
            if (hh == 0) lds_st<f32x2>(xch + 1024 + th * 256, f32x2{ps.r, ps.i});
            BARRIER();
            const f32x2 o = lds_ld<f32x2>(xch + 1024 + (th ^ 1) * 256);
            carry = {ps.r + o.x, ps.i + o.y};
        }
        STAMP(4);
        const cpx F = cmul(Sl, cfma(lseg, carry, pre));
        if (c == 0) WAITV(0);
        {
            const unsigned hs = w * 8192;
            const unsigned gsw = ((ch >> 1) & 1) << 2 | ((ch >> 3) & 1) << 3 | (ch & 1) | ((ch >> 2) & 1) << 1;
            const unsigned wb = hs + ch * 128;
#pragma unroll
            for (int q = 0; q < 8; ++q) {
                f32x2 hr[2], hi[2];
#pragma unroll
                for (int e = 0; e < 2; ++e) {
                    const int t = 4 * q + 2 * e;
                    const f32x2 ur = {aR[t >> 4][t & 15], aR[t >> 4][(t & 15) + 1]}, ui = {aI[t >> 4][t & 15], aI[t >> 4][(t & 15) + 1]};
                    const cp2 qq = Q[t >> 1];
                    hr[e] = ur + qq.r * F.r - qq.i * F.i;
                    hi[e] = -ui - qq.r * F.i - qq.i * F.r;
                }
                const unsigned a = wb + (((8 * hh + q) ^ gsw) * 8);
                lds_st<v2u>(a, v2u{pk2v(hr[0]), pk2v(hr[1])});
                lds_st<v2u>(a + 4096, v2u{pk2v(hi[0]), pk2v(hi[1])});
            }
            const int gq = lane >> 4, ra = (lane >> 2) & 3, rp = lane & 3;
            unsigned rad[2];
#pragma unroll
            for (int hf = 0; hf < 2; ++hf) {
                const int row = 8 * gq + 4 * hf + ra;
                const unsigned gs = ((row >> 1) & 1) << 2 | ((row >> 3) & 1) << 3 | (row & 1) | ((row >> 2) & 1) << 1;
                rad[hf] = hs + row * 128 + ((rp ^ gs) * 8);
            }
            const unsigned hso = WS_H + ((2 * c + th) * 40 + 4 * g + cg) * 4096;
            v4u hq[8];
#pragma unroll
            for (int i = 0; i < 8; ++i) {
                const v2u x0 = lds_tr((rad[0] ^ ((i & 3) * 32)) + (i >> 2) * 4096), x1 = lds_tr((rad[1] ^ ((i & 3) * 32)) + (i >> 2) * 4096);
                hq[i] = v4u{x0.x, x0.y, x1.x, x1.y};
            }
            SB();
#pragma unroll
            for (int i = 0; i < 8; ++i)
                __builtin_amdgcn_raw_buffer_store_b128(hq[i], ws, lane * 16 + (i & 3) * 1024, hso + (i >> 2) * 8192, AUX_SC1);
            SB();
        }
#pragma unroll
        for (int f = 0; f < 10; ++f) { c_read(cs, cf, 1, f, cb1); c_read(cs, cf, 2, f, cb0); }
        asm volatile("s_waitcnt lgkmcnt(0)" ::: "memory");
#pragma unroll
        for (int i = 0; i < 10; ++i) c_dma(cs, 3, i, cb1);
        asm volatile("s_waitcnt vmcnt(10) lgkmcnt(0)\n\ts_barrier" ::: "memory");
        set_flag(ws, FL_H + c * 10 + g, tA, tB);
        STAMP(5);
        MEMFENCE();
#pragma unroll
        for (int i = 0; i < 10; ++i) c_dma(cs, 4, i, cb0);
        cbuf3 = cb1; cbuf4 = cb0;
    } else if (r < 30) {
        const int jj = r - 20, grp = xl * 10 + jj, H = xl >> 2, tb = 2 * (xl & 3) + H;
        const unsigned stg = w * STG_BYTES, kexp = KEXP_BASE + w * 2 * KEXP_CH;
        KgIn in[2]; v2u ep;
        {
            const rsrc_t r0 = mk(p.sare, SBYTES), r1 = mk(p.saim, SBYTES), r2 = mk(p.sbre, SBYTES), r3 = mk(p.sbim, SBYTES),
                         r4 = mk(p.scre, SBYTES), r5 = mk(p.scim, SBYTES), r6 = mk(p.sldt, VBYTES);
            const unsigned d0 = grp * 8 + 2 * w;
#pragma unroll
            for (int e = 0; e < 2; ++e)
                asm volatile("s_nop 4\n\tbuffer_load_dword %0, %7, %8, %15 offen\n\tbuffer_load_dword %1, %7, %9, %15 offen\n\t"
                             "buffer_load_dword %2, %7, %10, %15 offen\n\tbuffer_load_dword %3, %7, %11, %15 offen\n\t"
                             "buffer_load_dword %4, %7, %12, %15 offen\n\tbuffer_load_dword %5, %7, %13, %15 offen\n\t"
                             "buffer_load_ushort %6, off, %14, %16"
                             : "=&v"(in[e].are), "=&v"(in[e].aim), "=&v"(in[e].bre), "=&v"(in[e].bim), "=&v"(in[e].cre), "=&v"(in[e].cim), "=&v"(in[e].ldt)
                             : "v"(lane * 4), "s"(r0), "s"(r1), "s"(r2), "s"(r3), "s"(r4), "s"(r5), "s"(r6), "s"((d0 + e) * 256), "s"((d0 + e) * 2)
                             : "memory");
            asm volatile("buffer_load_dwordx2 %0, off, %1, 0 sc1" : "=&v"(ep) : "s"(ws) : "memory");
        }
        {
            const int rb = lane >> 3;
#pragma unroll
            for (int i = 0; i < 8; ++i) {
                const unsigned f = (((rb >> 1) & 1) | ((i & 1) << 1)) << 1;
                dma16(kexp + i * 1024, rb * 1280 + (((lane & 7) ^ f) * 16), rx, (256 * tb + 64 * w + 8 * i) * 1280 + jj * 128);
            }
        }
        const rsrc_t rpf = mk(jj < 5 ? p.bre : p.bim, MBYTES);
        const unsigned pfo = (320 * H + 64 * (jj < 5 ? jj : jj - 5)) * 1280;
        touch(lane * 1280, rpf, pfo + w * 128);
        asm volatile("s_waitcnt vmcnt(9)" : "+v"(in[0].are), "+v"(in[0].aim), "+v"(in[0].bre), "+v"(in[0].bim), "+v"(in[0].cre), "+v"(in[0].cim), "+v"(in[0].ldt),
                     "+v"(in[1].are), "+v"(in[1].aim), "+v"(in[1].bre), "+v"(in[1].bim), "+v"(in[1].cre), "+v"(in[1].cim), "+v"(in[1].ldt), "+v"(ep)::"memory");
        tA = rfl(ep.x) + 1; tB = rfl(ep.y) + 1;
        STAMP(1);
        KgState ks = kg_lo(in[0], stg, lane);
        WAITV(1);
        {
            const int g4 = lane >> 4, ra = (lane >> 2) & 3, rp = lane & 3;
            v4u xq[8];
#pragma unroll
            for (int tk = 0; tk < 2; ++tk)
#pragma unroll
                for (int cs = 0; cs < 4; ++cs) {
                    v2u x[2];
#pragma unroll
                    for (int hf = 0; hf < 2; ++hf) {
                        const int row = 32 * tk + 8 * g4 + 4 * hf + ra;
                        const unsigned f = (((row >> 1) & 1) | (((row >> 3) & 1) << 1)) << 1;
                        x[hf] = lds_tr(kexp + row * 128 + (((2 * cs + (rp >> 1)) ^ f) * 16) + (rp & 1) * 8);
                    }
                    xq[tk * 4 + cs] = v4u{x[0].x, x[0].y, x[1].x, x[1].y};
                }
            SB();
            const unsigned vo = ((lane >> 3) & 1) * 32768 + (lane & 7) * 64 + g4 * 16;
#pragma unroll
            for (int i = 0; i < 8; ++i)
                __builtin_amdgcn_raw_buffer_store_b128(xq[i], ws, vo, WS_XT + (jj * 8 + 2 * (i & 3)) * 32768 + (tb * 8 + 2 * w + (i >> 2)) * 512, AUX_SC1);
            SB();
        }
        MEMFENCE();
        touch(lane * 1280, rpf, pfo + (w + 4) * 128);
        touch(lane * 1280, rpf, pfo + (w < 2 ? w + 8 : w + 4) * 128);
        STAMP(2);
        unsigned ga[32];
        {
            const unsigned tabm = stg + TAB_OFF - 2 * 0x4280;
#pragma unroll
            for (int rr = 0; rr < 4; ++rr)
#pragma unroll
                for (int e = 0; e < 4; ++e) {
                    const int l0 = 2044 + (lane & 3) - 32 * (16 * rr + (lane >> 2)) - 8 * e, l1 = l0 - 4;
                    const unsigned pk = pk2((float)l0, (float)l1);
                    unsigned g0 = tabm + 2 * (pk & 0xffffu), g1 = tabm + 2 * (pk >> 16);
                    if (rr == 3) {
                        if (l0 < 128) g0 = stg + TAB_OFF + 2 * l0;
                        if (l1 < 128) g1 = stg + TAB_OFF + 2 * l1;
                    }
                    ga[8 * rr + 2 * e] = g0; ga[8 * rr + 2 * e + 1] = g1;
                }
        }
        kg_hi(ks, stg, lane);
        kg_finish(stg, kexp, lane, ga);
        asm volatile("s_waitcnt vmcnt(2)\n\ts_barrier" ::: "memory");
        set_flag(ws, FL_XT + tb * 10 + jj, tA, tB);
        STAMP(3);
        ks = kg_lo(in[1], stg, lane);
        kg_hi(ks, stg, lane);
        SB();
        poll_flags(ws, WS_FLAG + (FL_XT + (lane & 7) * 10 + (grp >> 3)) * 256, tA, tB);
        v4u uu[8];
#pragma unroll
        for (int i = 0; i < 8; ++i)
            uu[i] = __builtin_amdgcn_raw_buffer_load_b128(ws, (lane >> 3) * 512 + (lane & 7) * 16, WS_XT + grp * 32768 + w * 128 + i * 4096, AUX_SC1);
        SB();
        kg_finish(stg, kexp + KEXP_CH, lane, ga);
        SB();
        {
            const unsigned ua = stg + ((lane >> 2) & 1) * U_CH + 64 + (lane >> 3) * 64 + (lane & 3) * 16;
#pragma unroll
            for (int i = 0; i < 8; ++i) lds_st<v4u>(ua + i * 512, uu[i]);
            if (lane < 8) lds_st<v4u>(stg + (lane >> 2) * U_CH + (lane & 3) * 16, v4u{0, 0, 0, 0});
        }
        STAMP(4);
        MEMFENCE();
        conv_channel<0>(kexp, stg, stg + YST_OFF, lane, cs, cf, stg + TAB_OFF);
        conv_channel<1>(kexp + KEXP_CH, stg + U_CH, stg + YST_OFF + 2, lane, cs, cf, stg + TAB_OFF);
        STAMP(5);
        {
            v4u yq[8];
#pragma unroll
            for (int i = 0; i < 8; ++i) yq[i] = lds_ld<v4u>(stg + YST_OFF + i * 1024 + lane * 16);
            SB();
#pragma unroll
            for (int i = 0; i < 8; ++i)
                __builtin_amdgcn_raw_buffer_store_b128(yq[i], ws, lane * 16, WS_Y + (grp * 4 + w) * 8192 + i * 1024, AUX_SC1);
        }
        asm volatile("s_waitcnt vmcnt(0) lgkmcnt(0)\n\ts_barrier" ::: "memory");
        set_flag(ws, FL_Y + grp, tA, tB);
        cbuf3 = 32768 + w * 10240; cbuf4 = 98304 + w * 10240;
        MEMFENCE();
#pragma unroll
        for (int i = 0; i < 10; ++i) c_dma(cs, 4, i, cbuf4);
    } else {
        const int gc = 2 * (2 * (xl & 3) + 1 - (xl >> 2)) + (r - 30);
        const v2u ep = __builtin_amdgcn_raw_buffer_load_b64(ws, WS_EPOCH, 0, AUX_SC1);
        const rsrc_t rgw = mk(p.gw, VBYTES);
        const unsigned vo = (lane >> 3) * 1280 + (lane & 7) * 16, so = (128 * gc + 32 * w) * 1280;
        float sum[4] = {0.f, 0.f, 0.f, 0.f};
        v4u gv[10], xv[10][4];
#pragma unroll
        for (int s = 0; s < 10; ++s) {
            gv[s] = __builtin_amdgcn_raw_buffer_load_b128(rgw, (lane & 7) * 16, s * 128, 0);
#pragma unroll
            for (int q = 0; q < 4; ++q) xv[s][q] = __builtin_amdgcn_raw_buffer_load_b128(rx, vo, so + q * 8 * 1280 + s * 128, 0);
        }
        SB();
#pragma unroll
        for (int s = 0; s < 10; ++s)
#pragma unroll
            for (int e = 0; e < 4; ++e) {
                const f32x2 gw2 = {bflo(gv[s][e]), bfhi(gv[s][e])};
#pragma unroll
                for (int q = 0; q < 4; ++q) {
                    const unsigned pr = pk2v(f32x2{bflo(xv[s][q][e]), bfhi(xv[s][q][e])} * gw2);
                    sum[q] += bflo(pr); sum[q] += bfhi(pr);
                }
            }
        tA = rfl(ep.x) + 1; tB = rfl(ep.y) + 1;
#pragma unroll
        for (int q = 0; q < 4; ++q) {
            float v = sum[q];
            v += __shfl_xor(v, 1); v += __shfl_xor(v, 2); v += __shfl_xor(v, 4);
            const float z = rbf(v), gt = rbf(1.f / (1.f + expf(-z)));
            if ((lane & 7) == 0)
                __builtin_amdgcn_raw_buffer_store_b32(__float_as_uint(gt), ws, WS_G + (128 * gc + 32 * w + 8 * q + (lane >> 3)) * 4, 0, AUX_SC1);
        }
        asm volatile("s_waitcnt vmcnt(0) lgkmcnt(0)\n\ts_barrier" ::: "memory");
        set_flag(ws, FL_G + gc, tA, tB);
        STAMP(5);
        MEMFENCE();
        cbuf3 = 32768 + w * 10240; cbuf4 = 98304 + w * 10240;
#pragma unroll
        for (int pb = 0; pb < 3; ++pb) {
#pragma unroll
            for (int i = 0; i < 10; ++i) c_dma(cs, pb, i, cbuf3);
            WAITV(0);
#pragma unroll
            for (int f = 0; f < 10; ++f) c_read(cs, cf, pb, f, cbuf3);
            asm volatile("s_waitcnt lgkmcnt(0)" ::: "memory");
        }
#pragma unroll
        for (int i = 0; i < 10; ++i) { c_dma(cs, 3, i, cbuf3); c_dma(cs, 4, i, cbuf4); }
    }

    const int own = (w + ct) & 3;
    const int tok = 64 * tt + 16 * own + (lane & 15), cq = lane >> 4;
    v2u xe[5], da[5], db[5];
    {
        const rsrc_t ra = mk(p.dska, VBYTES), rb = mk(p.dskb, VBYTES);
#pragma unroll
        for (int nt = 0; nt < 5; ++nt) {
            xe[nt] = __builtin_amdgcn_raw_buffer_load_b64(rx, tok * 1280 + cq * 8, ct * 160 + nt * 32, 0);
            da[nt] = __builtin_amdgcn_raw_buffer_load_b64(ra, cq * 8, ct * 160 + nt * 32, 0);
            db[nt] = __builtin_amdgcn_raw_buffer_load_b64(rb, cq * 8, ct * 160 + nt * 32, 0);
        }
    }
    {
        const int g0 = (10 * w) >> 2;
        poll_flags(ws, WS_FLAG + (lane < 3 ? FL_H + (tt >> 1) * 10 + g0 + lane : FL_G + (tt >> 1)) * 256, tA, tB);
    }
    STAMP(6);
    if (r < 20 || r >= 30) {
#pragma unroll
        for (int f = 0; f < 10; ++f) c_read(cs, cf, 3, f, cbuf3);
    }
#pragma unroll
    for (int f = 0; f < 10; ++f) c_read(cs, cf, 4, f, cbuf4);
    const float gate = __uint_as_float(__builtin_amdgcn_raw_buffer_load_b32(ws, WS_G + tok * 4, 0, AUX_SC1));
    f32x4 fin[5];
    {
        constexpr int WIN = 20;
        v4u hv[WIN];
        const unsigned hb = WS_H + (tt * 40 + 10 * w) * 4096;
        unsigned so[4];
#pragma unroll
        for (int o = 0; o < 4; ++o) so[o] = hb + ((own + 1 + o) & 3) * 1024;
#pragma unroll
        for (int i = 0; i < WIN; ++i) hv[i] = __builtin_amdgcn_raw_buffer_load_b128(ws, lane * 16, so[i / 10] + (i % 10) * 4096, AUX_SC1);
        SB();
#pragma unroll
        for (int o = 0; o < 4; ++o) {
            f32x4 acc[5];
#pragma unroll
            for (int j = 0; j < 10; ++j) {
                const int i = 10 * o + j;
                const v4u hf = hv[i % WIN];
#pragma unroll
                for (int nt = 0; nt < 5; ++nt) {
                    if (j == 0) MFMA16AZ(acc[nt], cf[nt], hf); else MFMA16A(acc[nt], cf[5 * j + nt], hf);
                }
                if (i + WIN < 40)
                    hv[i % WIN] = __builtin_amdgcn_raw_buffer_load_b128(ws, lane * 16, so[(i + WIN) / 10] + ((i + WIN) % 10) * 4096, AUX_SC1);
                SB();
            }
            asm volatile("s_nop 7" : "+v"(acc[0]), "+v"(acc[1]), "+v"(acc[2]), "+v"(acc[3]), "+v"(acc[4]));
            if (o < 3) {
                const unsigned ra = red_off(((own + 1 + o) & 3) * 3 + o) + lane * 16;
#pragma unroll
                for (int nt = 0; nt < 5; ++nt) lds_st<f32x4>(ra + nt * 1024, acc[nt]);
            } else {
#pragma unroll
                for (int nt = 0; nt < 5; ++nt) fin[nt] = acc[nt];
            }
            SB();
        }
    }
    STAMP(7);
    poll_flags(ws, WS_FLAG + (FL_Y + ct * 10 + (lane < 10 ? lane : 0)) * 256, tA, tB);
    unsigned yv[5][2];
#pragma unroll
    for (int nt = 0; nt < 5; ++nt)
#pragma unroll
        for (int e = 0; e < 2; ++e)
            yv[nt][e] = __builtin_amdgcn_raw_buffer_load_b32(ws, tok * 4 + cq * 16384, WS_Y + (40 * ct + 8 * nt + e) * 8192, AUX_SC1);
    BARRIER();
#pragma unroll
    for (int o = 0; o < 3; ++o)
#pragma unroll
        for (int nt = 0; nt < 5; ++nt) fin[nt] += lds_ld<f32x4>(red_off(own * 3 + o) + lane * 16 + nt * 1024);
    STAMP(8);
    {
        const rsrc_t ro = mk(p.out, SEQ * DM * 4);
        const float omg = rbf(1.f - gate);
        f32x4 oo[5];
#pragma unroll
        for (int nt = 0; nt < 5; ++nt) {
            f32x4 o;
#pragma unroll
            for (int e = 0; e < 2; ++e) {
                const f32x2 x2 = {bflo(xe[nt][e]), bfhi(xe[nt][e])};
                const unsigned sa = pk2v(x2 * f32x2{bflo(da[nt][e]), bfhi(da[nt][e])});
                const unsigned sb = pk2v(x2 * f32x2{bflo(db[nt][e]), bfhi(db[nt][e])});
                const unsigned bb = pk2v(f32x2{bflo(yv[nt][e]), bfhi(yv[nt][e])} + f32x2{bflo(sb), bfhi(sb)});
                const unsigned ob = pk2v(f32x2{bflo(bb), bfhi(bb)} * omg);
                const float a0 = fin[nt][2 * e] + bflo(sa), a1 = fin[nt][2 * e + 1] + bfhi(sa);
                o[2 * e] = __fadd_rn(__fmul_rn(gate, a0), bflo(ob));
                o[2 * e + 1] = __fadd_rn(__fmul_rn(gate, a1), bfhi(ob));
            }
            oo[nt] = o;
        }
        SB();
#pragma unroll
        for (int nt = 0; nt < 5; ++nt)
            __builtin_amdgcn_raw_buffer_store_b128(__builtin_bit_cast(v4u, oo[nt]), ro, tok * 2560 + cq * 16, ct * 320 + nt * 64, 2);
        asm volatile("s_nop 1");
        SB();
    }
    if (b == 255 && w == 0) {
        for (int k = 0; k < 4; ++k) {
            const int i = k * 64 + lane;
            poll_flags(ws, WS_FLAG + (i < 240 ? i : i + 80) * 256, tA, tB);
        }
        if (lane == 0) __builtin_amdgcn_raw_buffer_store_b64(v2u{tA, tB}, ws, WS_EPOCH, 0, AUX_SC1);
    }
}

extern "C" void kernel_launch(void* const* d_in, const int* in_sizes, int n_in, void* d_out, int out_size, void* d_ws,
                              size_t ws_size, hipStream_t stream) {
    Params p;
    p.x = d_in[0]; p.lre = d_in[1]; p.lim = d_in[2]; p.bre = d_in[3]; p.bim = d_in[4]; p.cre = d_in[5]; p.cim = d_in[6];
    p.ldt = d_in[7]; p.dska = d_in[8];
    p.sare = d_in[10]; p.saim = d_in[11]; p.sbre = d_in[12]; p.sbim = d_in[13]; p.scre = d_in[14]; p.scim = d_in[15];
    p.sldt = d_in[16]; p.dskb = d_in[17]; p.gw = d_in[18];
    p.out = d_out; p.ws = d_ws;
    (void)hipFuncSetAttribute((const void*)k_fused, hipFuncAttributeMaxDynamicSharedMemorySize, LDS_BYTES);
    k_fused<<<256, 256, LDS_BYTES, stream>>>(p);
}
```

```cpp
#define DIAG 1
#define RWG 160
#define RFROM 0
#define RTO 0
#define RCYC 0
#include <hip/hip_runtime.h>
#include <stdint.h>

#define DI __device__ __forceinline__
#define GAS __attribute__((address_space(1)))
#define LAS __attribute__((address_space(3)))
typedef unsigned short u16;
typedef unsigned long long u64;
typedef unsigned v4u __attribute__((ext_vector_type(4)));
typedef unsigned v2u __attribute__((ext_vector_type(2)));
typedef float f32x2 __attribute__((ext_vector_type(2)));
typedef float f32x4 __attribute__((ext_vector_type(4)));
typedef float f32x16 __attribute__((ext_vector_type(16)));
typedef short s16x4 __attribute__((ext_vector_type(4)));
typedef __bf16 bf16x2 __attribute__((ext_vector_type(2)));
typedef __amdgpu_buffer_rsrc_t rsrc_t;

constexpr int SEQ = 2048, DM = 640, NMODE = 128;
constexpr unsigned XBYTES = SEQ * DM * 2, MBYTES = DM * DM * 2, SBYTES = DM * NMODE * 2, VBYTES = DM * 2;

constexpr unsigned WS_EPOCH = 0;
constexpr unsigned WS_FLAG = 4096;
constexpr unsigned FL_H = 0, FL_XT = 160, FL_Y = 240, FL_G = 320, FL_N = 336;
constexpr unsigned WS_G = 96 * 1024;
constexpr unsigned WS_AGG = 128 * 1024;
constexpr unsigned WS_H = 512 * 1024;
constexpr unsigned WS_XT = WS_H + SEQ * 1280 * 2;
constexpr unsigned WS_Y = WS_XT + XBYTES;
constexpr unsigned WS_END = WS_Y + XBYTES;
constexpr unsigned WS_STAMP = 16u << 20;

constexpr int LDS_BYTES = 163840;
constexpr unsigned LDS_DUMMY = LDS_BYTES - 256;
constexpr int SPIN_CAP = 20000;
constexpr int AUX_SC1 = 16;

#define NC1 20
#define NC2 10

struct Params {
    const void *x, *lre, *lim, *bre, *bim, *cre, *cim, *ldt, *dska;
    const void *sare, *saim, *sbre, *sbim, *scre, *scim, *sldt, *dskb, *gw;
    void *out, *ws;
};

DI rsrc_t mk(const void* p, unsigned bytes) { return __builtin_amdgcn_make_buffer_rsrc((void*)p, (short)0, (int)bytes, 0x00020000); }
DI float bflo(unsigned v) { return __uint_as_float(v << 16); }
DI float bfhi(unsigned v) { return __uint_as_float(v & 0xffff0000u); }
DI unsigned pk2(float a, float b) { f32x2 v = {a, b}; return __builtin_bit_cast(unsigned, __builtin_convertvector(v, bf16x2)); }
DI unsigned pk2v(f32x2 v) { return __builtin_bit_cast(unsigned, __builtin_convertvector(v, bf16x2)); }
DI float rbf(float a) { return bflo(pk2(a, a)); }
DI unsigned rfl(unsigned v) { return __builtin_amdgcn_readfirstlane(v); }
#define SB() __builtin_amdgcn_sched_barrier(0)
#define MEMFENCE() asm volatile("" ::: "memory")

template <class T> DI T lds_ld(unsigned a) { return *(const LAS T*)(uintptr_t)a; }
template <class T> DI void lds_st(unsigned a, T v) { *(LAS T*)(uintptr_t)a = v; }
DI v2u lds_tr(unsigned a) { return __builtin_bit_cast(v2u, __builtin_amdgcn_ds_read_tr16_b64_v4i16((LAS s16x4*)(uintptr_t)a)); }

#define MFMA32(acc, a, b) asm volatile("v_mfma_f32_32x32x16_bf16 %0, %1, %2, %0" : "+v"(acc) : "v"(a), "v"(b))
#define MFMA32Z(acc, a, b) asm volatile("v_mfma_f32_32x32x16_bf16 %0, %1, %2, 0" : "=&v"(acc) : "v"(a), "v"(b))
#define MFMA16A(acc, a, b) asm volatile("v_mfma_f32_16x16x32_bf16 %0, %1, %2, %0" : "+v"(acc) : "a"(a), "v"(b))
#define MFMA16AZ(acc, a, b) asm volatile("v_mfma_f32_16x16x32_bf16 %0, %1, %2, 0" : "=&v"(acc) : "a"(a), "v"(b))

DI void dma16(unsigned ldsoff, unsigned voff, rsrc_t rs, unsigned soff) {
    asm volatile("s_mov_b32 m0, %0\n\ts_nop 0\n\tbuffer_load_dwordx4 %1, %2, %3 offen lds" ::"s"(ldsoff), "v"(voff), "s"(rs), "s"(soff) : "memory");
}
DI void touch(unsigned voff, rsrc_t rs, unsigned soff) {
    asm volatile("s_mov_b32 m0, %0\n\ts_nop 0\n\tbuffer_load_dword %1, %2, %3 offen lds" ::"s"(LDS_DUMMY), "v"(voff), "s"(rs), "s"(soff) : "memory");
}
#define WAITV(n) asm volatile("s_waitcnt vmcnt(%0)" ::"n"(n) : "memory")
#define BARRIER() asm volatile("s_waitcnt lgkmcnt(0)\n\ts_barrier" ::: "memory")

#define STAMP(i) do { SB(); if (w == 0) { u64 t_, c_; asm volatile("s_memrealtime %0\n\ts_memtime %1\n\ts_waitcnt lgkmcnt(0)" : "=s"(t_), "=s"(c_)::"memory"); \
    lds_st<v4u>(163328 + (i) * 16, v4u{(unsigned)t_, (unsigned)(t_ >> 32), (unsigned)c_, (unsigned)(c_ >> 32)}); asm volatile("s_waitcnt lgkmcnt(0)" ::: "memory"); } SB(); } while (0)

DI void poll_flags(rsrc_t ws, unsigned off, unsigned tA, unsigned tB) {
    for (int spin = 0; spin < SPIN_CAP; ++spin) {
        MEMFENCE();
        const v2u f = __builtin_amdgcn_raw_buffer_load_b64(ws, off, 0, AUX_SC1);
        if (__all((f.x == tA) & (f.y == tB))) break;
    }
    __builtin_amdgcn_fence(__ATOMIC_ACQUIRE, "wavefront");
    MEMFENCE();
}
DI void set_flag(rsrc_t ws, unsigned idx, unsigned tA, unsigned tB) {
    if (threadIdx.x == 0) __builtin_amdgcn_raw_buffer_store_b64(v2u{tA, tB}, ws, WS_FLAG + idx * 256, 0, AUX_SC1);
}

struct cpx { float r, i; };
DI cpx cmul(cpx a, cpx b) { return {a.r * b.r - a.i * b.i, a.r * b.i + a.i * b.r}; }
DI cpx cfma(cpx a, cpx b, cpx c) { return {a.r * b.r - a.i * b.i + c.r, a.r * b.i + a.i * b.r + c.i}; }
struct cp2 { f32x2 r, i; };
DI cp2 cmul2(cp2 a, cp2 b) { return {a.r * b.r - a.i * b.i, a.r * b.i + a.i * b.r}; }
DI cp2 cmul2s(cp2 a, cpx b) { return {a.r * b.r - a.i * b.i, a.r * b.i + a.i * b.r}; }
DI void halves(float v, float& lo, float& hi) {
    auto s = __builtin_amdgcn_permlane32_swap(__float_as_uint(v), __float_as_uint(v), false, false);
    lo = __uint_as_float(s[0]); hi = __uint_as_float(s[1]);
}

struct CStage { rsrc_t r0, r1; unsigned vo[2], ra[2]; int w; };
DI void c_dma(const CStage& c, int pb, int i, unsigned buf) {
    const unsigned u = 5u * c.w + pb;
    dma16(buf + i * 1024, c.vo[i & 1], (pb & 1) ? c.r1 : c.r0, (u >> 1) * 128u + i * 8u * 1280u);
}
DI void c_read(const CStage& c, v4u (&cf)[50], int pb, int f, unsigned buf) {
    cf[10 * pb + f] = lds_ld<v4u>(buf + c.ra[f / 5] + (f % 5) * 2048);
}

struct KgIn { unsigned are, aim, bre, bim, cre, cim, ldt; };
DI cp2 cexp2(f32x2 zr2, f32x2 zi2, float l) {
    const f32x2 a = zr2 * l, b = zi2 * l;
    cp2 o;
    const float m0 = __builtin_amdgcn_exp2f(a.x), m1 = __builtin_amdgcn_exp2f(a.y);
    const float f0 = __builtin_amdgcn_fractf(b.x), f1 = __builtin_amdgcn_fractf(b.y);
    o.r = f32x2{m0 * __builtin_amdgcn_cosf(f0), m1 * __builtin_amdgcn_cosf(f1)};
    o.i = f32x2{m0 * __builtin_amdgcn_sinf(f0), m1 * __builtin_amdgcn_sinf(f1)};
    return o;
}
constexpr unsigned STG_ROW = 528, STG_BYTES = 53 * STG_ROW, KEXP_BASE = 4 * STG_BYTES, KEXP_RHO = 1216, KEXP_CH = 4 * KEXP_RHO;
constexpr unsigned U_CH = 4160, YST_OFF = 2 * U_CH, TAB_OFF = YST_OFF + 8192;
DI void put_lo(unsigned a, int row, cp2 v) { lds_st<v2u>(a + (21 + row) * STG_ROW, v2u{pk2v(v.r), pk2v(v.i)}); }
DI void put_hi(unsigned a, int row, cp2 v) { lds_st<v2u>(a + row * STG_ROW, v2u{pk2v(v.r), pk2v(-v.i)}); }

struct KgState { f32x2 zr2, zi2; cp2 w; };
DI KgState kg_lo(const KgIn& in, unsigned stg, int lane) {
    const float dt = rbf(__expf(bflo(in.ldt)));
    KgState s;
    const f32x2 zr = f32x2{bflo(in.are), bfhi(in.are)} * dt, zi = f32x2{bflo(in.aim), bfhi(in.aim)} * dt;
    s.zr2 = zr * 1.44269504088896341f;
    s.zi2 = zi * 0.15915494309189535f;
    const cp2 db = {f32x2{bflo(in.bre), bfhi(in.bre)} * dt, f32x2{bflo(in.bim), bfhi(in.bim)} * dt};
    const cp2 cc = {f32x2{bflo(in.cre), bfhi(in.cre)}, f32x2{bflo(in.cim), bfhi(in.cim)}};
    s.w = cmul2(cc, db);
    const unsigned a = stg + lane * 8;
    const cp2 e8 = cexp2(s.zr2, s.zi2, 8.f), e32 = cexp2(s.zr2, s.zi2, 32.f);
    cp2 v0 = {f32x2{1.f, 1.f}, f32x2{0.f, 0.f}}, v1 = e8, v2 = cmul2(e8, e8), v3 = cmul2(v2, e8);
    put_lo(a, 0, v0); put_lo(a, 1, v1); put_lo(a, 2, v2); put_lo(a, 3, v3);
    v0 = e32;
#pragma unroll
    for (int k = 1; k < 8; ++k) {
        if (k > 1) v0 = cmul2(v0, e32);
        v1 = cmul2(v1, e32); v2 = cmul2(v2, e32); v3 = cmul2(v3, e32);
        put_lo(a, 4 * k, v0); put_lo(a, 4 * k + 1, v1); put_lo(a, 4 * k + 2, v2); put_lo(a, 4 * k + 3, v3);
    }
    return s;
}
DI void kg_hi(const KgState& s, unsigned stg, int lane) {
    const unsigned a = stg + lane * 8;
    const cp2 e1 = cexp2(s.zr2, s.zi2, 1.f), e256 = cexp2(s.zr2, s.zi2, 256.f);
    const cp2 e2 = cmul2(e1, e1), e4 = cmul2(e2, e2);
    cp2 w0 = s.w, w1 = cmul2(w0, e1), w2 = cmul2(w0, e2), w3 = cmul2(w1, e2);
    put_hi(a, 0, w0); put_hi(a, 1, w1); put_hi(a, 2, w2); put_hi(a, 3, w3);
    put_hi(a, 4, cmul2(w0, e4)); put_hi(a, 5, cmul2(w1, e4)); put_hi(a, 6, cmul2(w2, e4)); put_hi(a, 7, cmul2(w3, e4));
    cp2 q = cmul2(w0, e256);
    const cp2 q2 = cmul2(q, e2);
    put_hi(a, 8, q); put_hi(a, 9, q2); put_hi(a, 10, cmul2(q, e4)); put_hi(a, 11, cmul2(q2, e4));
    q = cmul2(q, e256); put_hi(a, 12, q); put_hi(a, 13, cmul2(q, e4));
    q = cmul2(q, e256); put_hi(a, 14, q); put_hi(a, 15, cmul2(q, e4));
#pragma unroll
    for (int m = 0; m < 5; ++m) { q = cmul2(q, e256); put_hi(a, 16 + m, q); }
}
DI void kg_finish(unsigned stg, unsigned kexp, int lane, const unsigned (&ga)[32]) {
    const int j = lane & 31, hh = lane >> 5;
    const unsigned ra = stg + j * STG_ROW + hh * 16;
    f32x16 d;
#pragma unroll
    for (int ks = 0; ks < 16; ++ks) {
        const v4u a = lds_ld<v4u>(ra + ks * 32), b = lds_ld<v4u>(ra + 21 * STG_ROW + ks * 32);
        if (ks == 0) MFMA32Z(d, a, b); else MFMA32(d, a, b);
    }
    asm volatile("s_nop 11" : "+v"(d));
    const unsigned tab = stg + TAB_OFF;
    lds_st<v2u>(tab + (8 * j + 4 * hh) * 2, v2u{pk2(d[0], d[1]), pk2(d[2], d[3])});
    const unsigned a1 = tab + (hh ? 384 + 2 * j : 256 + 4 * j) * 2, a2 = tab + (hh ? 448 + 2 * j : 258 + 4 * j) * 2;
    lds_st<unsigned>(a1, pk2(d[4], d[5]));
    lds_st<unsigned>(a2, pk2(d[6], d[7]));
    const unsigned a3 = tab + (hh ? 640 + j : 512 + j) * 2;
    const unsigned p89 = pk2(d[8], d[9]), pab = pk2(d[10], d[11]);
    lds_st<u16>(a3, (u16)p89); lds_st<u16>(a3 + 64, (u16)(p89 >> 16));
    lds_st<u16>(a3 + 128, (u16)pab); lds_st<u16>(a3 + 192, (u16)(pab >> 16));
    const unsigned wa = kexp + (lane & 3) * KEXP_RHO + (lane >> 2) * 16;
#pragma unroll
    for (int r = 0; r < 4; ++r) {
        v4u o;
#pragma unroll
        for (int e = 0; e < 4; ++e) {
            const unsigned lo = lds_ld<u16>(ga[8 * r + 2 * e]), hi = lds_ld<u16>(ga[8 * r + 2 * e + 1]);
            o[e] = lo | (hi << 16);
        }
        lds_st<v4u>(wa + r * 256, o);
    }
    if (lane < 28) lds_st<v4u>(wa + 1024, v4u{0, 0, 0, 0});
}

#define SHADD(d, prev, cur, y) asm volatile("v_add_f32_dpp %0, %1, %3 wave_ror:1 row_mask:0xf bank_mask:0xf\n\t" \
    "v_add_f32_dpp %0, %2, %3 wave_shr:1 row_mask:0xf bank_mask:0xf" : "=&v"(d) : "v"(prev), "v"(cur), "v"(y))
#define SHADD0(d, cur, y) asm volatile("v_add_f32_dpp %0, %1, %2 wave_shr:1 row_mask:0xf bank_mask:0xf bound_ctrl:0" : "=&v"(d) : "v"(cur), "v"(y))

template <int E>
DI void conv_channel(unsigned kexp, unsigned ub, unsigned yst, int lane, const CStage& cs, v4u (&cf)[50], unsigned cbuf) {
    const int hh = lane >> 5;
    const unsigned aA = kexp + (lane & 3) * KEXP_RHO + (56 - 8 * ((lane & 31) >> 2) + 8 * hh) * 2;
    const unsigned aB = ub + 2 * (4 + 32 * hh + 4 * ((lane >> 2) & 3) + 16 * ((lane >> 4) & 1) + 4 * (lane & 3));
    f32x16 acc[8];
    v4u af[2][8];
#pragma unroll
    for (int dl = 0; dl < 8; ++dl) af[0][dl] = lds_ld<v4u>(aA + (64 * (7 - dl)) * 2);
    v2u b0 = lds_tr(aB), b1 = lds_tr(aB + 32);
#pragma unroll
    for (int c = 0; c < 4; ++c) {
#pragma unroll
        for (int i = 0; i < 8; ++i) {
            const v4u bf = {b0.x, b0.y, b1.x, b1.y};
            const int ni = (i + 1) & 7, nc = c + (i == 7);
            if (nc < 4) { b0 = lds_tr(aB + 128 * (4 * ni + nc)); b1 = lds_tr(aB + 128 * (4 * ni + nc) + 32); }
            if (c < 3) af[(c + 1) & 1][i] = lds_ld<v4u>(aA + (64 * (7 - i) + 16 * (c + 1)) * 2);
            SB();
#pragma unroll
            for (int dl = 0; dl < 8 - i; ++dl) {
                if (c == 0 && i == 0) MFMA32Z(acc[dl], af[0][dl], bf);
                else MFMA32(acc[i + dl], af[c & 1][dl], bf);
                const int m = 36 * c + 8 * i - i * (i - 1) / 2 + dl, pb = 2 * E + m / 72, mm = m % 72;
                if (mm < 40 && (mm & 3) == 0) { SB(); c_dma(cs, pb, mm >> 2, cbuf); SB(); }
                if (mm == 59) { SB(); WAITV(0); SB(); }
                if (mm >= 60 && mm < 70) { SB(); c_read(cs, cf, pb, mm - 60, cbuf); SB(); }
                if (mm == 71) { SB(); asm volatile("s_waitcnt lgkmcnt(0)" ::: "memory"); SB(); }
            }
            SB();
        }
    }
    asm volatile("s_nop 11" : "+v"(acc[7]));
    float p2[32], p1[32], y[32];
#pragma unroll
    for (int G = 0; G < 32; ++G) {
        const float cur = acc[G >> 2][4 * (G & 3) + 3], yy = acc[G >> 2][4 * (G & 3) + 2];
        if (G == 0) SHADD0(p2[0], cur, yy);
        else { const float prev = acc[(G - 1) >> 2][4 * ((G - 1) & 3) + 3]; SHADD(p2[G], prev, cur, yy); }
    }
#pragma unroll
    for (int G = 0; G < 32; ++G) {
        const float yy = acc[G >> 2][4 * (G & 3) + 1];
        if (G == 0) SHADD0(p1[0], p2[0], yy); else SHADD(p1[G], p2[G - 1], p2[G], yy);
    }
#pragma unroll
    for (int G = 0; G < 32; ++G) {
        const float yy = acc[G >> 2][4 * (G & 3)];
        if (G == 0) SHADD0(y[0], p1[0], yy); else SHADD(y[G], p1[G - 1], p1[G], yy);
    }
    const unsigned ya = yst + lane * 4;
#pragma unroll
    for (int G = 0; G < 32; G += 2) {
        const unsigned pk = pk2(y[G], y[G + 1]);
        lds_st<u16>(ya + G * 256, (u16)pk);
        lds_st<u16>(ya + G * 256 + 256, (u16)(pk >> 16));
    }
}

DI unsigned red_off(int i) { return i * 5120 + (i >= 6 ? 43008 : 0) + (i >= 10 ? 45056 : 0); }

__global__ __launch_bounds__(256, 1) void k_fused(Params p) {
    const int tid = threadIdx.x, lane = tid & 63;
    const int w = __builtin_amdgcn_readfirstlane(tid >> 6);
    const int b = blockIdx.x, xl = b & 7, r = b >> 3;
    if (tid < 16) lds_st<v4u>(163328 + tid * 16, v4u{0, 0, 0, 0});
    STAMP(0);
    const rsrc_t ws = mk(p.ws, WS_END);
    const rsrc_t rx = mk(p.x, XBYTES);
    const int rn = r < 20 ? r : r - 20;
    const int tti = rn & 3, kk5 = rn >> 2;
    const int ct = (2 * tti + (r < 20 ? (kk5 < 2 ? kk5 + 2 : kk5 + 3) : (kk5 < 2 ? kk5 : 4))) & 7;
    const int tt = 4 * xl + tti;
    CStage cs;
    cs.r0 = mk((w & 1) ? p.cim : p.cre, MBYTES); cs.r1 = mk((w & 1) ? p.cre : p.cim, MBYTES); cs.w = w;
#pragma unroll
    for (int e = 0; e < 2; ++e) {
        cs.vo[e] = (80 * ct + (lane >> 3)) * 1280 + (((lane & 7) ^ ((4 * e + (lane >> 4)) & 7)) * 16);
        cs.ra[e] = (lane & 15) * 128 + (((4 * e + (lane >> 4)) ^ ((lane >> 1) & 7)) * 16);
    }
    v4u cf[50];
    unsigned tA, tB, cbuf3, cbuf4;

    if (r < 20) {
        const int c = 4 * (xl & 3) + (r & 3), g = 5 * (xl >> 2) + (r >> 2);
        const int th = w & 1, cg = w >> 1, ch = lane & 31, hh = lane >> 5;
        const rsrc_t rbre = mk(p.bre, MBYTES), rbim = mk(p.bim, MBYTES);
        const int pch = 64 * g + 32 * cg + ch;
        unsigned vlr, vli, vld; v2u ep;
        {
            const rsrc_t r0 = mk(p.lre, VBYTES), r1 = mk(p.lim, VBYTES), r2 = mk(p.ldt, VBYTES);
            asm volatile("s_nop 4\n\tbuffer_load_ushort %0, %4, %5, 0 offen\n\tbuffer_load_ushort %1, %4, %6, 0 offen\n\t"
                         "buffer_load_ushort %2, %4, %7, 0 offen\n\tbuffer_load_dwordx2 %3, off, %8, 0 sc1"
                         : "=&v"(vlr), "=&v"(vli), "=&v"(vld), "=&v"(ep) : "v"(pch * 2), "s"(r0), "s"(r1), "s"(r2), "s"(ws) : "memory");
        }
        const int rb = lane >> 3;
        const unsigned gch = ((lane & 7) ^ ((4 * w + (rb >> 1)) & 7)) * 16;
        const unsigned vox = (32 * (rb >> 2) + (rb & 3)) * 1280 + gch, vob = rb * 1280 + gch;
        const unsigned sox = (128 * c + 4 * w) * 1280, sob = (64 * g + 8 * w) * 1280;
        auto dma_piece = [&](int ti, unsigned slot, unsigned ko) {
            const unsigned l = slot + ti * 4096 + w * 1024;
            if (ti < 4) dma16(l, vox, rx, sox + (64 * (ti >> 1) + 16 * (ti & 1)) * 1280 + ko);
            else dma16(l, vob, (ti & 1) ? rbim : rbre, sob + 32 * ((ti - 4) >> 1) * 1280 + ko);
        };
#pragma unroll
        for (int ti = 0; ti < 8; ++ti) dma_piece(ti, 0, 0);
        asm volatile("s_waitcnt vmcnt(8)" : "+v"(vlr), "+v"(vli), "+v"(vld), "+v"(ep)::"memory");
        tA = rfl(ep.x) + 1; tB = rfl(ep.y) + 1;
        cpx lam, S2;
        {
            const float dt = rbf(__expf(bflo(vld))), Lr = bflo(vlr), Li = bflo(vli);
            const float e = __expf(dt * Lr), fr = __builtin_amdgcn_fractf((dt * Li) * 0.15915494309189535f);
            lam = {e * __builtin_amdgcn_cosf(fr), e * __builtin_amdgcn_sinf(fr)};
            const float nr = lam.r - 1.f, ni = lam.i, den = 2.f * __builtin_amdgcn_rcpf(Lr * Lr + Li * Li);
            S2 = {(nr * Lr + ni * Li) * den, (ni * Lr - nr * Li) * den};
        }
        cp2 Q[16];
        Q[0] = {f32x2{1.f, lam.r}, f32x2{0.f, lam.i}};
        const cpx l2 = cmul(lam, lam), l4 = cmul(l2, l2), l8 = cmul(l4, l4), l16 = cmul(l8, l8), l32 = cmul(l16, l16);
        SB(); dma_piece(0, 32768, 128); dma_piece(1, 32768, 128); SB();
        Q[1] = cmul2s(Q[0], l2);
#pragma unroll
        for (int i = 0; i < 2; ++i) Q[2 + i] = cmul2s(Q[i], l4);
        SB(); dma_piece(2, 32768, 128); SB();
#pragma unroll
        for (int i = 0; i < 4; ++i) Q[4 + i] = cmul2s(Q[i], l8);
        SB(); dma_piece(3, 32768, 128); SB();
#pragma unroll
        for (int i = 0; i < 8; ++i) { Q[8 + i] = cmul2s(Q[i], l16); if ((i & 1) == 1) { SB(); dma_piece(4 + (i >> 1), 32768, 128); SB(); } }
        const cpx l64 = cmul(l32, l32), l128 = cmul(l64, l64), l256 = cmul(l128, l128), l512 = cmul(l256, l256);
        const int seg = 2 * th + hh;
        cpx lseg = {1.f, 0.f}, lgat = {1.f, 0.f};
        if (hh) { lseg = l32; lgat = l128; }
        if (th) { lseg = cmul(lseg, l64); lgat = cmul(lgat, l256); }
        const cpx Sl = cmul(S2, lam);

        f32x16 aR[2], aI[2];
#pragma unroll
        for (int i = 0; i < 16; ++i) { aR[0][i] = 0.f; aR[1][i] = 0.f; aI[0][i] = 0.f; aI[1][i] = 0.f; }
        unsigned adA[4], adB[4];
        {
            const unsigned lb = ch * 128 + ((hh ^ ((ch >> 1) & 7)) * 16);
#pragma unroll
            for (int k = 0; k < 4; ++k) { adA[k] = (lb ^ (k * 32)) + th * 8192; adB[k] = (lb ^ (k * 32)) + 16384 + cg * 8192; }
        }
        STAMP(1);
        asm volatile("s_waitcnt vmcnt(8)\n\ts_barrier" : "+v"(aR[0]), "+v"(aI[0]), "+v"(aR[1]), "+v"(aI[1])::"memory");
        v4u fa0 = lds_ld<v4u>(adA[0]), fa1 = lds_ld<v4u>(adA[0] + 4096), fr = lds_ld<v4u>(adB[0]), fi = lds_ld<v4u>(adB[0] + 4096);
        const unsigned cb0 = 98304 + w * 10240, cb1 = 32768 + w * 10240;
#pragma unroll
        for (int s = 0; s < 10; ++s) {
#pragma unroll
            for (int k = 0; k < 4; ++k) {
                v4u na0 = fa0, na1 = fa1, nr = fr, ni = fi;
                if (k == 3) {
                    if (s <= 8) asm volatile("s_waitcnt vmcnt(6) lgkmcnt(0)\n\ts_barrier" ::: "memory");
                }
                const int kn = (k + 1) & 3;
                const unsigned so = ((k == 3 ? s + 1 : s) % 3) * 32768;
                const bool rd = !(k == 3 && s == 9);
                MFMA32(aR[0], fa0, fr); SB();
                if (rd) { na0 = lds_ld<v4u>(adA[kn] + so); nr = lds_ld<v4u>(adB[kn] + so); } SB();
                MFMA32(aI[0], fa0, fi); SB();
                if (rd) { ni = lds_ld<v4u>(adB[kn] + so + 4096); na1 = lds_ld<v4u>(adA[kn] + so + 4096); } SB();
                MFMA32(aR[1], fa1, fr);
                if (s <= 7) dma_piece(2 * k, ((s + 2) % 3) * 32768, (s + 2) * 128);
                else if (s == 8) c_dma(cs, 0, 2 * k, cb0);
                else if (k == 0) c_dma(cs, 0, 8, cb0);
                MFMA32(aI[1], fa1, fi);
                if (s <= 7) dma_piece(2 * k + 1, ((s + 2) % 3) * 32768, (s + 2) * 128);
                else if (s == 8) c_dma(cs, 0, 2 * k + 1, cb0);
                else if (k == 0) c_dma(cs, 0, 9, cb0);
                SB();
                fa0 = na0; fa1 = na1; fr = nr; fi = ni;
            }
        }
        asm volatile("s_nop 11" : "+v"(aR[0]), "+v"(aI[0]), "+v"(aR[1]), "+v"(aI[1]));
        STAMP(2);
        cpx agg;
        {
            f32x2 sr0 = {0.f, 0.f}, si0 = {0.f, 0.f}, sr1 = {0.f, 0.f}, si1 = {0.f, 0.f};
#pragma unroll
            for (int i = 0; i < 8; ++i) {
                const f32x2 ur = {aR[0][2 * i], aR[0][2 * i + 1]}, ui = {aI[0][2 * i], aI[0][2 * i + 1]};
                const f32x2 qr = Q[15 - i].r.yx, qi = Q[15 - i].i.yx;
                sr0 += qr * ur; si0 += qr * ui; sr0 -= qi * ui; si0 += qi * ur;
                const f32x2 vr = {aR[1][2 * i], aR[1][2 * i + 1]}, vi = {aI[1][2 * i], aI[1][2 * i + 1]};
                const f32x2 pr = Q[7 - i].r.yx, pi = Q[7 - i].i.yx;
                sr1 += pr * vr; si1 += pr * vi; sr1 -= pi * vi; si1 += pi * vr;
            }
            sr0 += sr1; si0 += si1;
            agg = {sr0.x + sr0.y, si0.x + si0.y};
        }
        cpx a0, a1;
        halves(agg.r, a0.r, a1.r); halves(agg.i, a0.i, a1.i);
        const cpx a64 = cfma(l32, a0, a1);
        const unsigned xch = 159744 + (cg * 2) * 256 + ch * 8;
        if (hh == 0) lds_st<f32x2>(xch + th * 256, f32x2{a64.r, a64.i});
        BARRIER();
        cpx pre = {0.f, 0.f};
        if (th) {
            const f32x2 o = lds_ld<f32x2>(xch);
            const cpx o64 = {o.x, o.y};
            const cpx a128 = cfma(l64, o64, a64);
            if (hh == 0 && c < 15)
                __builtin_amdgcn_raw_buffer_store_b128(v4u{__float_as_uint(a128.r), tA, __float_as_uint(a128.i), tB}, ws,
                                                       WS_AGG + (c * 640 + pch) * 16, 0, AUX_SC1);
            pre = hh ? cfma(l32, o64, a0) : o64;
        } else if (hh) pre = a0;
        STAMP(3);
        {
            cpx v = {0.f, 0.f};
#pragma unroll
            for (int t = 0; t < 32; ++t) {
                v = cfma(lam, v, cpx{aR[t >> 4][t & 15], aI[t >> 4][t & 15]});
                const cpx o = cmul(S2, v);
                aR[t >> 4][t & 15] = o.r; aI[t >> 4][t & 15] = o.i;
                SB();
                if (t < 10) c_dma(cs, 1, t, cb1);
                if (t == 10) { if (th && c < 15) WAITV(11); else WAITV(10); }
                if (t >= 10 && t < 20) c_read(cs, cf, 0, t - 10, cb0);
                if (t == 21) asm volatile("s_waitcnt lgkmcnt(0)" ::: "memory");
                if (t >= 21 && t < 31) c_dma(cs, 2, t - 21, cb0);
                SB();
            }
        }
        asm volatile("" : "+v"(aR[0]), "+v"(aI[0]), "+v"(aR[1]), "+v"(aI[1]));
        cpx carry = {0.f, 0.f};
        if (c > 0) {
            v4u ga[4];
            for (int spin = 0; spin < SPIN_CAP; ++spin) {
                MEMFENCE();
                bool ok = true;
#pragma unroll
                for (int i = 0; i < 4; ++i) {
                    const int j = c - 1 - (4 * i + seg);
                    ga[i] = __builtin_amdgcn_raw_buffer_load_b128(ws, WS_AGG + ((j < 0 ? 0 : j) * 640 + pch) * 16, 0, AUX_SC1);
                }
#pragma unroll
                for (int i = 0; i < 4; ++i) ok = ok & ((c - 1 - (4 * i + seg) < 0) | ((ga[i].y == tA) & (ga[i].w == tB)));
                if (__all(ok)) break;
            }
            cpx part = {0.f, 0.f};
#pragma unroll
            for (int i = 3; i >= 0; --i) {
                const bool val = c - 1 - (4 * i + seg) >= 0;
                const cpx a = {val ? __uint_as_float(ga[i].x) : 0.f, val ? __uint_as_float(ga[i].z) : 0.f};
                part = cfma(l512, part, a);
            }
            part = cmul(lgat, part);
            cpx p0, p1;
            halves(part.r, p0.r, p1.r); halves(part.i, p0.i, p1.i);
            const cpx ps = {p0.r + p1.r, p0.i + p1.i};
            if (hh == 0) lds_st<f32x2>(xch + 1024 + th * 256, f32x2{ps.r, ps.i});
            BARRIER();
            const f32x2 o = lds_ld<f32x2>(xch + 1024 + (th ^ 1) * 256);
            carry = {ps.r + o.x, ps.i + o.y};
        }
        STAMP(4);
        const cpx F = cmul(Sl, cfma(lseg, carry, pre));
        if (c == 0) WAITV(0);
        {
            const unsigned hs = w * 8192;
            const unsigned gsw = ((ch >> 1) & 1) << 2 | ((ch >> 3) & 1) << 3 | (ch & 1) | ((ch >> 2) & 1) << 1;
            const unsigned wb = hs + ch * 128;
#pragma unroll
            for (int q = 0; q < 8; ++q) {
                f32x2 hr[2], hi[2];
#pragma unroll
                for (int e = 0; e < 2; ++e) {
                    const int t = 4 * q + 2 * e;
                    const f32x2 ur = {aR[t >> 4][t & 15], aR[t >> 4][(t & 15) + 1]}, ui = {aI[t >> 4][t & 15], aI[t >> 4][(t & 15) + 1]};
                    const cp2 qq = Q[t >> 1];
                    hr[e] = ur + qq.r * F.r - qq.i * F.i;
                    hi[e] = -ui - qq.r * F.i - qq.i * F.r;
                }
                const unsigned a = wb + (((8 * hh + q) ^ gsw) * 8);
                lds_st<v2u>(a, v2u{pk2v(hr[0]), pk2v(hr[1])});
                lds_st<v2u>(a + 4096, v2u{pk2v(hi[0]), pk2v(hi[1])});
            }
            const int gq = lane >> 4, ra = (lane >> 2) & 3, rp = lane & 3;
            unsigned rad[2];
#pragma unroll
            for (int hf = 0; hf < 2; ++hf) {
                const int row = 8 * gq + 4 * hf + ra;
                const unsigned gs = ((row >> 1) & 1) << 2 | ((row >> 3) & 1) << 3 | (row & 1) | ((row >> 2) & 1) << 1;
                rad[hf] = hs + row * 128 + ((rp ^ gs) * 8);
            }
            const unsigned hso = WS_H + ((2 * c + th) * 40 + 4 * g + cg) * 4096;
            v4u hq[8];
#pragma unroll
            for (int i = 0; i < 8; ++i) {
                const v2u x0 = lds_tr((rad[0] ^ ((i & 3) * 32)) + (i >> 2) * 4096), x1 = lds_tr((rad[1] ^ ((i & 3) * 32)) + (i >> 2) * 4096);
                hq[i] = v4u{x0.x, x0.y, x1.x, x1.y};
            }
            SB();
#pragma unroll
            for (int i = 0; i < 8; ++i)
                __builtin_amdgcn_raw_buffer_store_b128(hq[i], ws, lane * 16 + (i & 3) * 1024, hso + (i >> 2) * 8192, AUX_SC1);
            SB();
        }
#pragma unroll
        for (int f = 0; f < 10; ++f) { c_read(cs, cf, 1, f, cb1); c_read(cs, cf, 2, f, cb0); }
        asm volatile("s_waitcnt lgkmcnt(0)" ::: "memory");
#pragma unroll
        for (int i = 0; i < 10; ++i) c_dma(cs, 3, i, cb1);
        asm volatile("s_waitcnt vmcnt(10) lgkmcnt(0)\n\ts_barrier" ::: "memory");
        set_flag(ws, FL_H + c * 10 + g, tA, tB);
        STAMP(5);
        MEMFENCE();
#pragma unroll
        for (int i = 0; i < 10; ++i) c_dma(cs, 4, i, cb0);
        cbuf3 = cb1; cbuf4 = cb0;
    } else if (r < 30) {
        const int jj = r - 20, grp = xl * 10 + jj, H = xl >> 2, tb = 2 * (xl & 3) + H;
        const unsigned stg = w * STG_BYTES, kexp = KEXP_BASE + w * 2 * KEXP_CH;
        KgIn in[2]; v2u ep;
        {
            const rsrc_t r0 = mk(p.sare, SBYTES), r1 = mk(p.saim, SBYTES), r2 = mk(p.sbre, SBYTES), r3 = mk(p.sbim, SBYTES),
                         r4 = mk(p.scre, SBYTES), r5 = mk(p.scim, SBYTES), r6 = mk(p.sldt, VBYTES);
            const unsigned d0 = grp * 8 + 2 * w;
#pragma unroll
            for (int e = 0; e < 2; ++e)
                asm volatile("s_nop 4\n\tbuffer_load_dword %0, %7, %8, %15 offen\n\tbuffer_load_dword %1, %7, %9, %15 offen\n\t"
                             "buffer_load_dword %2, %7, %10, %15 offen\n\tbuffer_load_dword %3, %7, %11, %15 offen\n\t"
                             "buffer_load_dword %4, %7, %12, %15 offen\n\tbuffer_load_dword %5, %7, %13, %15 offen\n\t"
                             "buffer_load_ushort %6, off, %14, %16"
                             : "=&v"(in[e].are), "=&v"(in[e].aim), "=&v"(in[e].bre), "=&v"(in[e].bim), "=&v"(in[e].cre), "=&v"(in[e].cim), "=&v"(in[e].ldt)
                             : "v"(lane * 4), "s"(r0), "s"(r1), "s"(r2), "s"(r3), "s"(r4), "s"(r5), "s"(r6), "s"((d0 + e) * 256), "s"((d0 + e) * 2)
                             : "memory");
            asm volatile("buffer_load_dwordx2 %0, off, %1, 0 sc1" : "=&v"(ep) : "s"(ws) : "memory");
        }
        {
            const int rb = lane >> 3;
#pragma unroll
            for (int i = 0; i < 8; ++i) {
                const unsigned f = (((rb >> 1) & 1) | ((i & 1) << 1)) << 1;
                dma16(kexp + i * 1024, rb * 1280 + (((lane & 7) ^ f) * 16), rx, (256 * tb + 64 * w + 8 * i) * 1280 + jj * 128);
            }
        }
        const rsrc_t rpf = mk(jj < 5 ? p.bre : p.bim, MBYTES);
        const unsigned pfo = (320 * H + 64 * (jj < 5 ? jj : jj - 5)) * 1280;
        touch(lane * 1280, rpf, pfo + w * 128);
        asm volatile("s_waitcnt vmcnt(9)" : "+v"(in[0].are), "+v"(in[0].aim), "+v"(in[0].bre), "+v"(in[0].bim), "+v"(in[0].cre), "+v"(in[0].cim), "+v"(in[0].ldt),
                     "+v"(in[1].are), "+v"(in[1].aim), "+v"(in[1].bre), "+v"(in[1].bim), "+v"(in[1].cre), "+v"(in[1].cim), "+v"(in[1].ldt), "+v"(ep)::"memory");
        tA = rfl(ep.x) + 1; tB = rfl(ep.y) + 1;
        STAMP(1);
        KgState ks = kg_lo(in[0], stg, lane);
        WAITV(1);
        {
            const int g4 = lane >> 4, ra = (lane >> 2) & 3, rp = lane & 3;
            v4u xq[8];
#pragma unroll
            for (int tk = 0; tk < 2; ++tk)
#pragma unroll
                for (int cs = 0; cs < 4; ++cs) {
                    v2u x[2];
#pragma unroll
                    for (int hf = 0; hf < 2; ++hf) {
                        const int row = 32 * tk + 8 * g4 + 4 * hf + ra;
                        const unsigned f = (((row >> 1) & 1) | (((row >> 3) & 1) << 1)) << 1;
                        x[hf] = lds_tr(kexp + row * 128 + (((2 * cs + (rp >> 1)) ^ f) * 16) + (rp & 1) * 8);
                    }
                    xq[tk * 4 + cs] = v4u{x[0].x, x[0].y, x[1].x, x[1].y};
                }
            SB();
            const unsigned vo = ((lane >> 3) & 1) * 32768 + (lane & 7) * 64 + g4 * 16;
#pragma unroll
            for (int i = 0; i < 8; ++i)
                __builtin_amdgcn_raw_buffer_store_b128(xq[i], ws, vo, WS_XT + (jj * 8 + 2 * (i & 3)) * 32768 + (tb * 8 + 2 * w + (i >> 2)) * 512, AUX_SC1);
            SB();
        }
        MEMFENCE();
        touch(lane * 1280, rpf, pfo + (w + 4) * 128);
        touch(lane * 1280, rpf, pfo + (w < 2 ? w + 8 : w + 4) * 128);
        STAMP(2);
        unsigned ga[32];
        {
            const unsigned tabm = stg + TAB_OFF - 2 * 0x4280;
#pragma unroll
            for (int rr = 0; rr < 4; ++rr)
#pragma unroll
                for (int e = 0; e < 4; ++e) {
                    const int l0 = 2044 + (lane & 3) - 32 * (16 * rr + (lane >> 2)) - 8 * e, l1 = l0 - 4;
                    const unsigned pk = pk2((float)l0, (float)l1);
                    unsigned g0 = tabm + 2 * (pk & 0xffffu), g1 = tabm + 2 * (pk >> 16);
                    if (rr == 3) {
                        if (l0 < 128) g0 = stg + TAB_OFF + 2 * l0;
                        if (l1 < 128) g1 = stg + TAB_OFF + 2 * l1;
                    }
                    ga[8 * rr + 2 * e] = g0; ga[8 * rr + 2 * e + 1] = g1;
                }
        }
        kg_hi(ks, stg, lane);
        kg_finish(stg, kexp, lane, ga);
        asm volatile("s_waitcnt vmcnt(2)\n\ts_barrier" ::: "memory");
        set_flag(ws, FL_XT + tb * 10 + jj, tA, tB);
        STAMP(3);
        ks = kg_lo(in[1], stg, lane);
        kg_hi(ks, stg, lane);
        SB();
        poll_flags(ws, WS_FLAG + (FL_XT + (lane & 7) * 10 + (grp >> 3)) * 256, tA, tB);
        v4u uu[8];
#pragma unroll
        for (int i = 0; i < 8; ++i)
            uu[i] = __builtin_amdgcn_raw_buffer_load_b128(ws, (lane >> 3) * 512 + (lane & 7) * 16, WS_XT + grp * 32768 + w * 128 + i * 4096, AUX_SC1);
        SB();
        kg_finish(stg, kexp + KEXP_CH, lane, ga);
        SB();
        {
            const unsigned ua = stg + ((lane >> 2) & 1) * U_CH + 64 + (lane >> 3) * 64 + (lane & 3) * 16;
#pragma unroll
            for (int i = 0; i < 8; ++i) lds_st<v4u>(ua + i * 512, uu[i]);
            if (lane < 8) lds_st<v4u>(stg + (lane >> 2) * U_CH + (lane & 3) * 16, v4u{0, 0, 0, 0});
        }
        STAMP(4);
        MEMFENCE();
        conv_channel<0>(kexp, stg, stg + YST_OFF, lane, cs, cf, stg + TAB_OFF);
        conv_channel<1>(kexp + KEXP_CH, stg + U_CH, stg + YST_OFF + 2, lane, cs, cf, stg + TAB_OFF);
        STAMP(5);
        {
            v4u yq[8];
#pragma unroll
            for (int i = 0; i < 8; ++i) yq[i] = lds_ld<v4u>(stg + YST_OFF + i * 1024 + lane * 16);
            SB();
#pragma unroll
            for (int i = 0; i < 8; ++i)
                __builtin_amdgcn_raw_buffer_store_b128(yq[i], ws, lane * 16, WS_Y + (grp * 4 + w) * 8192 + i * 1024, AUX_SC1);
        }
        asm volatile("s_waitcnt vmcnt(0) lgkmcnt(0)\n\ts_barrier" ::: "memory");
        set_flag(ws, FL_Y + grp, tA, tB);
        cbuf3 = 32768 + w * 10240; cbuf4 = 98304 + w * 10240;
        MEMFENCE();
#pragma unroll
        for (int i = 0; i < 10; ++i) c_dma(cs, 4, i, cbuf4);
    } else {
        const int gc = 2 * (2 * (xl & 3) + 1 - (xl >> 2)) + (r - 30);
        const v2u ep = __builtin_amdgcn_raw_buffer_load_b64(ws, WS_EPOCH, 0, AUX_SC1);
        const rsrc_t rgw = mk(p.gw, VBYTES);
        const unsigned vo = (lane >> 3) * 1280 + (lane & 7) * 16, so = (128 * gc + 32 * w) * 1280;
        float sum[4] = {0.f, 0.f, 0.f, 0.f};
        v4u gv[10], xv[10][4];
#pragma unroll
        for (int s = 0; s < 10; ++s) {
            gv[s] = __builtin_amdgcn_raw_buffer_load_b128(rgw, (lane & 7) * 16, s * 128, 0);
#pragma unroll
            for (int q = 0; q < 4; ++q) xv[s][q] = __builtin_amdgcn_raw_buffer_load_b128(rx, vo, so + q * 8 * 1280 + s * 128, 0);
        }
        SB();
#pragma unroll
        for (int s = 0; s < 10; ++s)
#pragma unroll
            for (int e = 0; e < 4; ++e) {
                const f32x2 gw2 = {bflo(gv[s][e]), bfhi(gv[s][e])};
#pragma unroll
                for (int q = 0; q < 4; ++q) {
                    const unsigned pr = pk2v(f32x2{bflo(xv[s][q][e]), bfhi(xv[s][q][e])} * gw2);
                    sum[q] += bflo(pr); sum[q] += bfhi(pr);
                }
            }
        tA = rfl(ep.x) + 1; tB = rfl(ep.y) + 1;
#pragma unroll
        for (int q = 0; q < 4; ++q) {
            float v = sum[q];
            v += __shfl_xor(v, 1); v += __shfl_xor(v, 2); v += __shfl_xor(v, 4);
            const float z = rbf(v), gt = rbf(1.f / (1.f + expf(-z)));
            if ((lane & 7) == 0)
                __builtin_amdgcn_raw_buffer_store_b32(__float_as_uint(gt), ws, WS_G + (128 * gc + 32 * w + 8 * q + (lane >> 3)) * 4, 0, AUX_SC1);
        }
        asm volatile("s_waitcnt vmcnt(0) lgkmcnt(0)\n\ts_barrier" ::: "memory");
        set_flag(ws, FL_G + gc, tA, tB);
        STAMP(5);
        MEMFENCE();
        cbuf3 = 32768 + w * 10240; cbuf4 = 98304 + w * 10240;
#pragma unroll
        for (int pb = 0; pb < 3; ++pb) {
#pragma unroll
            for (int i = 0; i < 10; ++i) c_dma(cs, pb, i, cbuf3);
            WAITV(0);
#pragma unroll
            for (int f = 0; f < 10; ++f) c_read(cs, cf, pb, f, cbuf3);
            asm volatile("s_waitcnt lgkmcnt(0)" ::: "memory");
        }
#pragma unroll
        for (int i = 0; i < 10; ++i) { c_dma(cs, 3, i, cbuf3); c_dma(cs, 4, i, cbuf4); }
    }

    const int own = (w + ct) & 3;
    const int tok = 64 * tt + 16 * own + (lane & 15), cq = lane >> 4;
    v2u xe[5], da[5], db[5];
    {
        const rsrc_t ra = mk(p.dska, VBYTES), rb = mk(p.dskb, VBYTES);
#pragma unroll
        for (int nt = 0; nt < 5; ++nt) {
            xe[nt] = __builtin_amdgcn_raw_buffer_load_b64(rx, tok * 1280 + cq * 8, ct * 160 + nt * 32, 0);
            da[nt] = __builtin_amdgcn_raw_buffer_load_b64(ra, cq * 8, ct * 160 + nt * 32, 0);
            db[nt] = __builtin_amdgcn_raw_buffer_load_b64(rb, cq * 8, ct * 160 + nt * 32, 0);
        }
    }
    {
        const int g0 = (10 * w) >> 2;
        poll_flags(ws, WS_FLAG + (lane < 3 ? FL_H + (tt >> 1) * 10 + g0 + lane : FL_G + (tt >> 1)) * 256, tA, tB);
    }
    STAMP(6);
    if (r < 20 || r >= 30) {
#pragma unroll
        for (int f = 0; f < 10; ++f) c_read(cs, cf, 3, f, cbuf3);
    }
#pragma unroll
    for (int f = 0; f < 10; ++f) c_read(cs, cf, 4, f, cbuf4);
    const float gate = __uint_as_float(__builtin_amdgcn_raw_buffer_load_b32(ws, WS_G + tok * 4, 0, AUX_SC1));
    f32x4 fin[5];
    {
        constexpr int WIN = 20;
        v4u hv[WIN];
        const unsigned hb = WS_H + (tt * 40 + 10 * w) * 4096;
        unsigned so[4];
#pragma unroll
        for (int o = 0; o < 4; ++o) so[o] = hb + ((own + 1 + o) & 3) * 1024;
#pragma unroll
        for (int i = 0; i < WIN; ++i) hv[i] = __builtin_amdgcn_raw_buffer_load_b128(ws, lane * 16, so[i / 10] + (i % 10) * 4096, AUX_SC1);
        SB();
#pragma unroll
        for (int o = 0; o < 4; ++o) {
            f32x4 acc[5];
#pragma unroll
            for (int j = 0; j < 10; ++j) {
                const int i = 10 * o + j;
                const v4u hf = hv[i % WIN];
#pragma unroll
                for (int nt = 0; nt < 5; ++nt) {
                    if (j == 0) MFMA16AZ(acc[nt], cf[nt], hf); else MFMA16A(acc[nt], cf[5 * j + nt], hf);
                }
                if (i + WIN < 40)
                    hv[i % WIN] = __builtin_amdgcn_raw_buffer_load_b128(ws, lane * 16, so[(i + WIN) / 10] + ((i + WIN) % 10) * 4096, AUX_SC1);
                SB();
            }
            asm volatile("s_nop 7" : "+v"(acc[0]), "+v"(acc[1]), "+v"(acc[2]), "+v"(acc[3]), "+v"(acc[4]));
            if (o < 3) {
                const unsigned ra = red_off(((own + 1 + o) & 3) * 3 + o) + lane * 16;
#pragma unroll
                for (int nt = 0; nt < 5; ++nt) lds_st<f32x4>(ra + nt * 1024, acc[nt]);
            } else {
#pragma unroll
                for (int nt = 0; nt < 5; ++nt) fin[nt] = acc[nt];
            }
            SB();
        }
    }
    STAMP(7);
    poll_flags(ws, WS_FLAG + (FL_Y + ct * 10 + (lane < 10 ? lane : 0)) * 256, tA, tB);
    unsigned yv[5][2];
#pragma unroll
    for (int nt = 0; nt < 5; ++nt)
#pragma unroll
        for (int e = 0; e < 2; ++e)
            yv[nt][e] = __builtin_amdgcn_raw_buffer_load_b32(ws, tok * 4 + cq * 16384, WS_Y + (40 * ct + 8 * nt + e) * 8192, AUX_SC1);
    BARRIER();
#pragma unroll
    for (int o = 0; o < 3; ++o)
#pragma unroll
        for (int nt = 0; nt < 5; ++nt) fin[nt] += lds_ld<f32x4>(red_off(own * 3 + o) + lane * 16 + nt * 1024);
    STAMP(8);
    {
        const rsrc_t ro = mk(p.out, SEQ * DM * 4);
        const float omg = rbf(1.f - gate);
        f32x4 oo[5];
#pragma unroll
        for (int nt = 0; nt < 5; ++nt) {
            f32x4 o;
#pragma unroll
            for (int e = 0; e < 2; ++e) {
                const f32x2 x2 = {bflo(xe[nt][e]), bfhi(xe[nt][e])};
                const unsigned sa = pk2v(x2 * f32x2{bflo(da[nt][e]), bfhi(da[nt][e])});
                const unsigned sb = pk2v(x2 * f32x2{bflo(db[nt][e]), bfhi(db[nt][e])});
                const unsigned bb = pk2v(f32x2{bflo(yv[nt][e]), bfhi(yv[nt][e])} + f32x2{bflo(sb), bfhi(sb)});
                const unsigned ob = pk2v(f32x2{bflo(bb), bfhi(bb)} * omg);
                const float a0 = fin[nt][2 * e] + bflo(sa), a1 = fin[nt][2 * e + 1] + bfhi(sa);
                o[2 * e] = __fadd_rn(__fmul_rn(gate, a0), bflo(ob));
                o[2 * e + 1] = __fadd_rn(__fmul_rn(gate, a1), bfhi(ob));
            }
            oo[nt] = o;
        }
        SB();
#pragma unroll
        for (int nt = 0; nt < 5; ++nt)
            __builtin_amdgcn_raw_buffer_store_b128(__builtin_bit_cast(v4u, oo[nt]), ro, tok * 2560 + cq * 16, ct * 320 + nt * 64, 0);
        asm volatile("s_nop 1");
        SB();
    }
    if (b == 255 && w == 0) {
        for (int k = 0; k < 4; ++k) {
            const int i = k * 64 + lane;
            poll_flags(ws, WS_FLAG + (i < 240 ? i : i + 80) * 256, tA, tB);
        }
        if (lane == 0) __builtin_amdgcn_raw_buffer_store_b64(v2u{tA, tB}, ws, WS_EPOCH, 0, AUX_SC1);
    }
    asm volatile("s_waitcnt vmcnt(0)" ::: "memory");
    STAMP(9);
    if (tid < 16) ((GAS v4u*)((char*)p.ws + WS_STAMP))[blockIdx.x * 16 + tid] = lds_ld<v4u>(163328 + tid * 16);
    if (b == RWG && w == 0) {
        const v4u s0 = lds_ld<v4u>(163328 + RFROM * 16), s1 = lds_ld<v4u>(163328 + RTO * 16);
        unsigned d = RCYC ? (rfl(s1.z) - rfl(s0.z)) / 10 : (rfl(s1.x) - rfl(s0.x)) * 10;
        if (d > 100000u) d = 100000u;
        const u64 t0 = __builtin_amdgcn_s_memrealtime();
        while (__builtin_amdgcn_s_memrealtime() - t0 < d) __builtin_amdgcn_s_sleep(2);
    }
}

extern "C" void kernel_launch(void* const* d_in, const int* in_sizes, int n_in, void* d_out, int out_size, void* d_ws,
                              size_t ws_size, hipStream_t stream) {
    Params p;
    p.x = d_in[0]; p.lre = d_in[1]; p.lim = d_in[2]; p.bre = d_in[3]; p.bim = d_in[4]; p.cre = d_in[5]; p.cim = d_in[6];
    p.ldt = d_in[7]; p.dska = d_in[8];
    p.sare = d_in[10]; p.saim = d_in[11]; p.sbre = d_in[12]; p.sbim = d_in[13]; p.scre = d_in[14]; p.scim = d_in[15];
    p.sldt = d_in[16]; p.dskb = d_in[17]; p.gw = d_in[18];
    p.out = d_out; p.ws = d_ws;
    (void)hipFuncSetAttribute((const void*)k_fused, hipFuncAttributeMaxDynamicSharedMemorySize, LDS_BYTES);
    k_fused<<<256, 256, LDS_BYTES, stream>>>(p);
}
```

```cpp
#define DIAG 1
#define RWG 160
#define RFROM 0
#define RTO 0
#define RCYC 0
#include <hip/hip_runtime.h>
#include <stdint.h>

#define DI __device__ __forceinline__
#define GAS __attribute__((address_space(1)))
#define LAS __attribute__((address_space(3)))
typedef unsigned short u16;
typedef unsigned long long u64;
typedef unsigned v4u __attribute__((ext_vector_type(4)));
typedef unsigned v2u __attribute__((ext_vector_type(2)));
typedef float f32x2 __attribute__((ext_vector_type(2)));
typedef float f32x4 __attribute__((ext_vector_type(4)));
typedef float f32x16 __attribute__((ext_vector_type(16)));
typedef short s16x4 __attribute__((ext_vector_type(4)));
typedef __bf16 bf16x2 __attribute__((ext_vector_type(2)));
typedef __amdgpu_buffer_rsrc_t rsrc_t;

constexpr int SEQ = 2048, DM = 640, NMODE = 128;
constexpr unsigned XBYTES = SEQ * DM * 2, MBYTES = DM * DM * 2, SBYTES = DM * NMODE * 2, VBYTES = DM * 2;

constexpr unsigned WS_EPOCH = 0;
constexpr unsigned WS_FLAG = 4096;
constexpr unsigned FL_H = 0, FL_XT = 160, FL_Y = 240, FL_G = 320, FL_N = 336;
constexpr unsigned WS_G = 96 * 1024;
constexpr unsigned WS_AGG = 128 * 1024;
constexpr unsigned WS_H = 512 * 1024;
constexpr unsigned WS_XT = WS_H + SEQ * 1280 * 2;
constexpr unsigned WS_Y = WS_XT + XBYTES;
constexpr unsigned WS_END = WS_Y + XBYTES;
constexpr unsigned WS_STAMP = 16u << 20;

constexpr int LDS_BYTES = 163840;
constexpr unsigned LDS_DUMMY = LDS_BYTES - 256;
constexpr int SPIN_CAP = 20000;
constexpr int AUX_SC1 = 16;

#define NC1 20
#define NC2 10

struct Params {
    const void *x, *lre, *lim, *bre, *bim, *cre, *cim, *ldt, *dska;
    const void *sare, *saim, *sbre, *sbim, *scre, *scim, *sldt, *dskb, *gw;
    void *out, *ws;
};

DI rsrc_t mk(const void* p, unsigned bytes) { return __builtin_amdgcn_make_buffer_rsrc((void*)p, (short)0, (int)bytes, 0x00020000); }
DI float bflo(unsigned v) { return __uint_as_float(v << 16); }
DI float bfhi(unsigned v) { return __uint_as_float(v & 0xffff0000u); }
DI unsigned pk2(float a, float b) { f32x2 v = {a, b}; return __builtin_bit_cast(unsigned, __builtin_convertvector(v, bf16x2)); }
DI unsigned pk2v(f32x2 v) { return __builtin_bit_cast(unsigned, __builtin_convertvector(v, bf16x2)); }
DI float rbf(float a) { return bflo(pk2(a, a)); }
DI unsigned rfl(unsigned v) { return __builtin_amdgcn_readfirstlane(v); }
#define SB() __builtin_amdgcn_sched_barrier(0)
#define MEMFENCE() asm volatile("" ::: "memory")

template <class T> DI T lds_ld(unsigned a) { return *(const LAS T*)(uintptr_t)a; }
template <class T> DI void lds_st(unsigned a, T v) { *(LAS T*)(uintptr_t)a = v; }
DI v2u lds_tr(unsigned a) { return __builtin_bit_cast(v2u, __builtin_amdgcn_ds_read_tr16_b64_v4i16((LAS s16x4*)(uintptr_t)a)); }

#define MFMA32(acc, a, b) asm volatile("v_mfma_f32_32x32x16_bf16 %0, %1, %2, %0" : "+v"(acc) : "v"(a), "v"(b))
#define MFMA32Z(acc, a, b) asm volatile("v_mfma_f32_32x32x16_bf16 %0, %1, %2, 0" : "=&v"(acc) : "v"(a), "v"(b))
#define MFMA16A(acc, a, b) asm volatile("v_mfma_f32_16x16x32_bf16 %0, %1, %2, %0" : "+v"(acc) : "a"(a), "v"(b))
#define MFMA16AZ(acc, a, b) asm volatile("v_mfma_f32_16x16x32_bf16 %0, %1, %2, 0" : "=&v"(acc) : "a"(a), "v"(b))

DI void dma16(unsigned ldsoff, unsigned voff, rsrc_t rs, unsigned soff) {
    asm volatile("s_mov_b32 m0, %0\n\ts_nop 0\n\tbuffer_load_dwordx4 %1, %2, %3 offen lds" ::"s"(ldsoff), "v"(voff), "s"(rs), "s"(soff) : "memory");
}
DI void touch(unsigned voff, rsrc_t rs, unsigned soff) {
    asm volatile("s_mov_b32 m0, %0\n\ts_nop 0\n\tbuffer_load_dword %1, %2, %3 offen lds" ::"s"(LDS_DUMMY), "v"(voff), "s"(rs), "s"(soff) : "memory");
}
#define WAITV(n) asm volatile("s_waitcnt vmcnt(%0)" ::"n"(n) : "memory")
#define BARRIER() asm volatile("s_waitcnt lgkmcnt(0)\n\ts_barrier" ::: "memory")

#define STAMP(i) do { SB(); if (w == 0) { u64 t_, c_; asm volatile("s_memrealtime %0\n\ts_memtime %1\n\ts_waitcnt lgkmcnt(0)" : "=s"(t_), "=s"(c_)::"memory"); \
    lds_st<v4u>(163328 + (i) * 16, v4u{(unsigned)t_, (unsigned)(t_ >> 32), (unsigned)c_, (unsigned)(c_ >> 32)}); asm volatile("s_waitcnt lgkmcnt(0)" ::: "memory"); } SB(); } while (0)

DI void poll_flags(rsrc_t ws, unsigned off, unsigned tA, unsigned tB) {
    for (int spin = 0; spin < SPIN_CAP; ++spin) {
        MEMFENCE();
        const v2u f = __builtin_amdgcn_raw_buffer_load_b64(ws, off, 0, AUX_SC1);
        if (__all((f.x == tA) & (f.y == tB))) break;
    }
    __builtin_amdgcn_fence(__ATOMIC_ACQUIRE, "wavefront");
    MEMFENCE();
}
DI void set_flag(rsrc_t ws, unsigned idx, unsigned tA, unsigned tB) {
    if (threadIdx.x == 0) __builtin_amdgcn_raw_buffer_store_b64(v2u{tA, tB}, ws, WS_FLAG + idx * 256, 0, AUX_SC1);
}

struct cpx { float r, i; };
DI cpx cmul(cpx a, cpx b) { return {a.r * b.r - a.i * b.i, a.r * b.i + a.i * b.r}; }
DI cpx cfma(cpx a, cpx b, cpx c) { return {a.r * b.r - a.i * b.i + c.r, a.r * b.i + a.i * b.r + c.i}; }
struct cp2 { f32x2 r, i; };
DI cp2 cmul2(cp2 a, cp2 b) { return {a.r * b.r - a.i * b.i, a.r * b.i + a.i * b.r}; }
DI cp2 cmul2s(cp2 a, cpx b) { return {a.r * b.r - a.i * b.i, a.r * b.i + a.i * b.r}; }
DI void halves(float v, float& lo, float& hi) {
    auto s = __builtin_amdgcn_permlane32_swap(__float_as_uint(v), __float_as_uint(v), false, false);
    lo = __uint_as_float(s[0]); hi = __uint_as_float(s[1]);
}

struct CStage { rsrc_t r0, r1; unsigned vo[2], ra[2]; int w; };
DI void c_dma(const CStage& c, int pb, int i, unsigned buf) {
    const unsigned u = 5u * c.w + pb;
    dma16(buf + i * 1024, c.vo[i & 1], (pb & 1) ? c.r1 : c.r0, (u >> 1) * 128u + i * 8u * 1280u);
}
DI void c_read(const CStage& c, v4u (&cf)[50], int pb, int f, unsigned buf) {
    cf[10 * pb + f] = lds_ld<v4u>(buf + c.ra[f / 5] + (f % 5) * 2048);
}

struct KgIn { unsigned are, aim, bre, bim, cre, cim, ldt; };
DI cp2 cexp2(f32x2 zr2, f32x2 zi2, float l) {
    const f32x2 a = zr2 * l, b = zi2 * l;
    cp2 o;
    const float m0 = __builtin_amdgcn_exp2f(a.x), m1 = __builtin_amdgcn_exp2f(a.y);
    const float f0 = __builtin_amdgcn_fractf(b.x), f1 = __builtin_amdgcn_fractf(b.y);
    o.r = f32x2{m0 * __builtin_amdgcn_cosf(f0), m1 * __builtin_amdgcn_cosf(f1)};
    o.i = f32x2{m0 * __builtin_amdgcn_sinf(f0), m1 * __builtin_amdgcn_sinf(f1)};
    return o;
}
constexpr unsigned STG_ROW = 528, STG_BYTES = 53 * STG_ROW, KEXP_BASE = 4 * STG_BYTES, KEXP_RHO = 1216, KEXP_CH = 4 * KEXP_RHO;
constexpr unsigned U_CH = 4160, YST_OFF = 2 * U_CH, TAB_OFF = YST_OFF + 8192;
DI void put_lo(unsigned a, int row, cp2 v) { lds_st<v2u>(a + (21 + row) * STG_ROW, v2u{pk2v(v.r), pk2v(v.i)}); }
DI void put_hi(unsigned a, int row, cp2 v) { lds_st<v2u>(a + row * STG_ROW, v2u{pk2v(v.r), pk2v(-v.i)}); }

struct KgState { f32x2 zr2, zi2; cp2 w; };
DI KgState kg_lo(const KgIn& in, unsigned stg, int lane) {
    const float dt = rbf(__expf(bflo(in.ldt)));
    KgState s;
    const f32x2 zr = f32x2{bflo(in.are), bfhi(in.are)} * dt, zi = f32x2{bflo(in.aim), bfhi(in.aim)} * dt;
    s.zr2 = zr * 1.44269504088896341f;
    s.zi2 = zi * 0.15915494309189535f;
    const cp2 db = {f32x2{bflo(in.bre), bfhi(in.bre)} * dt, f32x2{bflo(in.bim), bfhi(in.bim)} * dt};
    const cp2 cc = {f32x2{bflo(in.cre), bfhi(in.cre)}, f32x2{bflo(in.cim), bfhi(in.cim)}};
    s.w = cmul2(cc, db);
    const unsigned a = stg + lane * 8;
    const cp2 e8 = cexp2(s.zr2, s.zi2, 8.f), e32 = cexp2(s.zr2, s.zi2, 32.f);
    cp2 v0 = {f32x2{1.f, 1.f}, f32x2{0.f, 0.f}}, v1 = e8, v2 = cmul2(e8, e8), v3 = cmul2(v2, e8);
    put_lo(a, 0, v0); put_lo(a, 1, v1); put_lo(a, 2, v2); put_lo(a, 3, v3);
    v0 = e32;
#pragma unroll
    for (int k = 1; k < 8; ++k) {
        if (k > 1) v0 = cmul2(v0, e32);
        v1 = cmul2(v1, e32); v2 = cmul2(v2, e32); v3 = cmul2(v3, e32);
        put_lo(a, 4 * k, v0); put_lo(a, 4 * k + 1, v1); put_lo(a, 4 * k + 2, v2); put_lo(a, 4 * k + 3, v3);
    }
    return s;
}
DI void kg_hi(const KgState& s, unsigned stg, int lane) {
    const unsigned a = stg + lane * 8;
    const cp2 e1 = cexp2(s.zr2, s.zi2, 1.f), e256 = cexp2(s.zr2, s.zi2, 256.f);
    const cp2 e2 = cmul2(e1, e1), e4 = cmul2(e2, e2);
    cp2 w0 = s.w, w1 = cmul2(w0, e1), w2 = cmul2(w0, e2), w3 = cmul2(w1, e2);
    put_hi(a, 0, w0); put_hi(a, 1, w1); put_hi(a, 2, w2); put_hi(a, 3, w3);
    put_hi(a, 4, cmul2(w0, e4)); put_hi(a, 5, cmul2(w1, e4)); put_hi(a, 6, cmul2(w2, e4)); put_hi(a, 7, cmul2(w3, e4));
    cp2 q = cmul2(w0, e256);
    const cp2 q2 = cmul2(q, e2);
    put_hi(a, 8, q); put_hi(a, 9, q2); put_hi(a, 10, cmul2(q, e4)); put_hi(a, 11, cmul2(q2, e4));
    q = cmul2(q, e256); put_hi(a, 12, q); put_hi(a, 13, cmul2(q, e4));
    q = cmul2(q, e256); put_hi(a, 14, q); put_hi(a, 15, cmul2(q, e4));
#pragma unroll
    for (int m = 0; m < 5; ++m) { q = cmul2(q, e256); put_hi(a, 16 + m, q); }
}
DI void kg_finish(unsigned stg, unsigned kexp, int lane, const unsigned (&ga)[32]) {
    const int j = lane & 31, hh = lane >> 5;
    const unsigned ra = stg + j * STG_ROW + hh * 16;
    f32x16 d;
#pragma unroll
    for (int ks = 0; ks < 16; ++ks) {
        const v4u a = lds_ld<v4u>(ra + ks * 32), b = lds_ld<v4u>(ra + 21 * STG_ROW + ks * 32);
        if (ks == 0) MFMA32Z(d, a, b); else MFMA32(d, a, b);
    }
    asm volatile("s_nop 11" : "+v"(d));
    const unsigned tab = stg + TAB_OFF;
    lds_st<v2u>(tab + (8 * j + 4 * hh) * 2, v2u{pk2(d[0], d[1]), pk2(d[2], d[3])});
    const unsigned a1 = tab + (hh ? 384 + 2 * j : 256 + 4 * j) * 2, a2 = tab + (hh ? 448 + 2 * j : 258 + 4 * j) * 2;
    lds_st<unsigned>(a1, pk2(d[4], d[5]));
    lds_st<unsigned>(a2, pk2(d[6], d[7]));
    const unsigned a3 = tab + (hh ? 640 + j : 512 + j) * 2;
    const unsigned p89 = pk2(d[8], d[9]), pab = pk2(d[10], d[11]);
    lds_st<u16>(a3, (u16)p89); lds_st<u16>(a3 + 64, (u16)(p89 >> 16));
    lds_st<u16>(a3 + 128, (u16)pab); lds_st<u16>(a3 + 192, (u16)(pab >> 16));
    const unsigned wa = kexp + (lane & 3) * KEXP_RHO + (lane >> 2) * 16;
#pragma unroll
    for (int r = 0; r < 4; ++r) {
        v4u o;
#pragma unroll
        for (int e = 0; e < 4; ++e) {
            const unsigned lo = lds_ld<u16>(ga[8 * r + 2 * e]), hi = lds_ld<u16>(ga[8 * r + 2 * e + 1]);
            o[e] = lo | (hi << 16);
        }
        lds_st<v4u>(wa + r * 256, o);
    }
    if (lane < 28) lds_st<v4u>(wa + 1024, v4u{0, 0, 0, 0});
}

#define SHADD(d, prev, cur, y) asm volatile("v_add_f32_dpp %0, %1, %3 wave_ror:1 row_mask:0xf bank_mask:0xf\n\t" \
    "v_add_f32_dpp %0, %2, %3 wave_shr:1 row_mask:0xf bank_mask:0xf" : "=&v"(d) : "v"(prev), "v"(cur), "v"(y))
#define SHADD0(d, cur, y) asm volatile("v_add_f32_dpp %0, %1, %2 wave_shr:1 row_mask:0xf bank_mask:0xf bound_ctrl:0" : "=&v"(d) : "v"(cur), "v"(y))

template <int E>
DI void conv_channel(unsigned kexp, unsigned ub, unsigned yst, int lane, const CStage& cs, v4u (&cf)[50], unsigned cbuf) {
    const int hh = lane >> 5;
    const unsigned aA = kexp + (lane & 3) * KEXP_RHO + (56 - 8 * ((lane & 31) >> 2) + 8 * hh) * 2;
    const unsigned aB = ub + 2 * (4 + 32 * hh + 4 * ((lane >> 2) & 3) + 16 * ((lane >> 4) & 1) + 4 * (lane & 3));
    f32x16 acc[8];
    v4u af[2][8];
#pragma unroll
    for (int dl = 0; dl < 8; ++dl) af[0][dl] = lds_ld<v4u>(aA + (64 * (7 - dl)) * 2);
    v2u b0 = lds_tr(aB), b1 = lds_tr(aB + 32);
#pragma unroll
    for (int c = 0; c < 4; ++c) {
#pragma unroll
        for (int i = 0; i < 8; ++i) {
            const v4u bf = {b0.x, b0.y, b1.x, b1.y};
            const int ni = (i + 1) & 7, nc = c + (i == 7);
            if (nc < 4) { b0 = lds_tr(aB + 128 * (4 * ni + nc)); b1 = lds_tr(aB + 128 * (4 * ni + nc) + 32); }
            if (c < 3) af[(c + 1) & 1][i] = lds_ld<v4u>(aA + (64 * (7 - i) + 16 * (c + 1)) * 2);
            SB();
#pragma unroll
            for (int dl = 0; dl < 8 - i; ++dl) {
                if (c == 0 && i == 0) MFMA32Z(acc[dl], af[0][dl], bf);
                else MFMA32(acc[i + dl], af[c & 1][dl], bf);
                const int m = 36 * c + 8 * i - i * (i - 1) / 2 + dl, pb = 2 * E + m / 72, mm = m % 72;
                if (mm < 40 && (mm & 3) == 0) { SB(); c_dma(cs, pb, mm >> 2, cbuf); SB(); }
                if (mm == 59) { SB(); WAITV(0); SB(); }
                if (mm >= 60 && mm < 70) { SB(); c_read(cs, cf, pb, mm - 60, cbuf); SB(); }
                if (mm == 71) { SB(); asm volatile("s_waitcnt lgkmcnt(0)" ::: "memory"); SB(); }
            }
            SB();
        }
    }
    asm volatile("s_nop 11" : "+v"(acc[7]));
    float p2[32], p1[32], y[32];
#pragma unroll
    for (int G = 0; G < 32; ++G) {
        const float cur = acc[G >> 2][4 * (G & 3) + 3], yy = acc[G >> 2][4 * (G & 3) + 2];
        if (G == 0) SHADD0(p2[0], cur, yy);
        else { const float prev = acc[(G - 1) >> 2][4 * ((G - 1) & 3) + 3]; SHADD(p2[G], prev, cur, yy); }
    }
#pragma unroll
    for (int G = 0; G < 32; ++G) {
        const float yy = acc[G >> 2][4 * (G & 3) + 1];
        if (G == 0) SHADD0(p1[0], p2[0], yy); else SHADD(p1[G], p2[G - 1], p2[G], yy);
    }
#pragma unroll
    for (int G = 0; G < 32; ++G) {
        const float yy = acc[G >> 2][4 * (G & 3)];
        if (G == 0) SHADD0(y[0], p1[0], yy); else SHADD(y[G], p1[G - 1], p1[G], yy);
    }
    const unsigned ya = yst + lane * 4;
#pragma unroll
    for (int G = 0; G < 32; G += 2) {
        const unsigned pk = pk2(y[G], y[G + 1]);
        lds_st<u16>(ya + G * 256, (u16)pk);
        lds_st<u16>(ya + G * 256 + 256, (u16)(pk >> 16));
    }
}

DI unsigned red_off(int i) { return i * 5120 + (i >= 6 ? 43008 : 0) + (i >= 10 ? 45056 : 0); }

__global__ __launch_bounds__(256, 1) void k_fused(Params p_) {
    typedef const __attribute__((address_space(4))) Params* kptr_t;
    kptr_t pp = (kptr_t)__builtin_amdgcn_kernarg_segment_ptr();
    u64 tfirst, tka;
    asm volatile("s_memrealtime %0\n\ts_waitcnt lgkmcnt(0)" : "=s"(tfirst), "+s"(pp)::"memory");
    Params p;
    p.x = pp->x; p.lre = pp->lre; p.lim = pp->lim; p.bre = pp->bre; p.bim = pp->bim; p.cre = pp->cre; p.cim = pp->cim; p.ldt = pp->ldt; p.dska = pp->dska;
    p.sare = pp->sare; p.saim = pp->saim; p.sbre = pp->sbre; p.sbim = pp->sbim; p.scre = pp->scre; p.scim = pp->scim; p.sldt = pp->sldt; p.dskb = pp->dskb; p.gw = pp->gw;
    p.out = pp->out; p.ws = pp->ws;
    asm volatile("s_waitcnt lgkmcnt(0)\n\ts_memrealtime %0\n\ts_waitcnt lgkmcnt(0)" : "=s"(tka), "+s"(p.x), "+s"(p.sare), "+s"(p.ws)::"memory");
    const int tid = threadIdx.x, lane = tid & 63;
    const int w = __builtin_amdgcn_readfirstlane(tid >> 6);
    const int b = blockIdx.x, xl = b & 7, r = b >> 3;
    if (tid < 16) lds_st<v4u>(163328 + tid * 16, v4u{0, 0, 0, 0});
    STAMP(0);
    const rsrc_t ws = mk(p.ws, WS_END);
    const rsrc_t rx = mk(p.x, XBYTES);
    const int rn = r < 20 ? r : r - 20;
    const int tti = rn & 3, kk5 = rn >> 2;
    const int ct = (2 * tti + (r < 20 ? (kk5 < 2 ? kk5 + 2 : kk5 + 3) : (kk5 < 2 ? kk5 : 4))) & 7;
    const int tt = 4 * xl + tti;
    CStage cs;
    cs.r0 = mk((w & 1) ? p.cim : p.cre, MBYTES); cs.r1 = mk((w & 1) ? p.cre : p.cim, MBYTES); cs.w = w;
#pragma unroll
    for (int e = 0; e < 2; ++e) {
        cs.vo[e] = (80 * ct + (lane >> 3)) * 1280 + (((lane & 7) ^ ((4 * e + (lane >> 4)) & 7)) * 16);
        cs.ra[e] = (lane & 15) * 128 + (((4 * e + (lane >> 4)) ^ ((lane >> 1) & 7)) * 16);
    }
    v4u cf[50];
    unsigned tA, tB, cbuf3, cbuf4;

    if (r < 20) {
        const int c = 4 * (xl & 3) + (r & 3), g = 5 * (xl >> 2) + (r >> 2);
        const int th = w & 1, cg = w >> 1, ch = lane & 31, hh = lane >> 5;
        const rsrc_t rbre = mk(p.bre, MBYTES), rbim = mk(p.bim, MBYTES);
        const int pch = 64 * g + 32 * cg + ch;
        unsigned vlr, vli, vld; v2u ep;
        {
            const rsrc_t r0 = mk(p.lre, VBYTES), r1 = mk(p.lim, VBYTES), r2 = mk(p.ldt, VBYTES);
            asm volatile("s_nop 4\n\tbuffer_load_ushort %0, %4, %5, 0 offen\n\tbuffer_load_ushort %1, %4, %6, 0 offen\n\t"
                         "buffer_load_ushort %2, %4, %7, 0 offen\n\tbuffer_load_dwordx2 %3, off, %8, 0 sc1"
                         : "=&v"(vlr), "=&v"(vli), "=&v"(vld), "=&v"(ep) : "v"(pch * 2), "s"(r0), "s"(r1), "s"(r2), "s"(ws) : "memory");
        }
        const int rb = lane >> 3;
        const unsigned gch = ((lane & 7) ^ ((4 * w + (rb >> 1)) & 7)) * 16;
        const unsigned vox = (32 * (rb >> 2) + (rb & 3)) * 1280 + gch, vob = rb * 1280 + gch;
        const unsigned sox = (128 * c + 4 * w) * 1280, sob = (64 * g + 8 * w) * 1280;
        auto dma_piece = [&](int ti, unsigned slot, unsigned ko) {
            const unsigned l = slot + ti * 4096 + w * 1024;
            if (ti < 4) dma16(l, vox, rx, sox + (64 * (ti >> 1) + 16 * (ti & 1)) * 1280 + ko);
            else dma16(l, vob, (ti & 1) ? rbim : rbre, sob + 32 * ((ti - 4) >> 1) * 1280 + ko);
        };
#pragma unroll
        for (int ti = 0; ti < 8; ++ti) dma_piece(ti, 0, 0);
        asm volatile("s_waitcnt vmcnt(8)" : "+v"(vlr), "+v"(vli), "+v"(vld), "+v"(ep)::"memory");
        tA = rfl(ep.x) + 1; tB = rfl(ep.y) + 1;
        cpx lam, S2;
        {
            const float dt = rbf(__expf(bflo(vld))), Lr = bflo(vlr), Li = bflo(vli);
            const float e = __expf(dt * Lr), fr = __builtin_amdgcn_fractf((dt * Li) * 0.15915494309189535f);
            lam = {e * __builtin_amdgcn_cosf(fr), e * __builtin_amdgcn_sinf(fr)};
            const float nr = lam.r - 1.f, ni = lam.i, den = 2.f * __builtin_amdgcn_rcpf(Lr * Lr + Li * Li);
            S2 = {(nr * Lr + ni * Li) * den, (ni * Lr - nr * Li) * den};
        }
        cp2 Q[16];
        Q[0] = {f32x2{1.f, lam.r}, f32x2{0.f, lam.i}};
        const cpx l2 = cmul(lam, lam), l4 = cmul(l2, l2), l8 = cmul(l4, l4), l16 = cmul(l8, l8), l32 = cmul(l16, l16);
        SB(); dma_piece(0, 32768, 128); dma_piece(1, 32768, 128); SB();
        Q[1] = cmul2s(Q[0], l2);
#pragma unroll
        for (int i = 0; i < 2; ++i) Q[2 + i] = cmul2s(Q[i], l4);
        SB(); dma_piece(2, 32768, 128); SB();
#pragma unroll
        for (int i = 0; i < 4; ++i) Q[4 + i] = cmul2s(Q[i], l8);
        SB(); dma_piece(3, 32768, 128); SB();
#pragma unroll
        for (int i = 0; i < 8; ++i) { Q[8 + i] = cmul2s(Q[i], l16); if ((i & 1) == 1) { SB(); dma_piece(4 + (i >> 1), 32768, 128); SB(); } }
        const cpx l64 = cmul(l32, l32), l128 = cmul(l64, l64), l256 = cmul(l128, l128), l512 = cmul(l256, l256);
        const int seg = 2 * th + hh;
        cpx lseg = {1.f, 0.f}, lgat = {1.f, 0.f};
        if (hh) { lseg = l32; lgat = l128; }
        if (th) { lseg = cmul(lseg, l64); lgat = cmul(lgat, l256); }
        const cpx Sl = cmul(S2, lam);

        f32x16 aR[2], aI[2];
#pragma unroll
        for (int i = 0; i < 16; ++i) { aR[0][i] = 0.f; aR[1][i] = 0.f; aI[0][i] = 0.f; aI[1][i] = 0.f; }
        unsigned adA[4], adB[4];
        {
            const unsigned lb = ch * 128 + ((hh ^ ((ch >> 1) & 7)) * 16);
#pragma unroll
            for (int k = 0; k < 4; ++k) { adA[k] = (lb ^ (k * 32)) + th * 8192; adB[k] = (lb ^ (k * 32)) + 16384 + cg * 8192; }
        }
        STAMP(1);
        asm volatile("s_waitcnt vmcnt(8)\n\ts_barrier" : "+v"(aR[0]), "+v"(aI[0]), "+v"(aR[1]), "+v"(aI[1])::"memory");
        v4u fa0 = lds_ld<v4u>(adA[0]), fa1 = lds_ld<v4u>(adA[0] + 4096), fr = lds_ld<v4u>(adB[0]), fi = lds_ld<v4u>(adB[0] + 4096);
        const unsigned cb0 = 98304 + w * 10240, cb1 = 32768 + w * 10240;
#pragma unroll
        for (int s = 0; s < 10; ++s) {
#pragma unroll
            for (int k = 0; k < 4; ++k) {
                v4u na0 = fa0, na1 = fa1, nr = fr, ni = fi;
                if (k == 3) {
                    if (s <= 8) asm volatile("s_waitcnt vmcnt(6) lgkmcnt(0)\n\ts_barrier" ::: "memory");
                }
                const int kn = (k + 1) & 3;
                const unsigned so = ((k == 3 ? s + 1 : s) % 3) * 32768;
                const bool rd = !(k == 3 && s == 9);
                MFMA32(aR[0], fa0, fr); SB();
                if (rd) { na0 = lds_ld<v4u>(adA[kn] + so); nr = lds_ld<v4u>(adB[kn] + so); } SB();
                MFMA32(aI[0], fa0, fi); SB();
                if (rd) { ni = lds_ld<v4u>(adB[kn] + so + 4096); na1 = lds_ld<v4u>(adA[kn] + so + 4096); } SB();
                MFMA32(aR[1], fa1, fr);
                if (s <= 7) dma_piece(2 * k, ((s + 2) % 3) * 32768, (s + 2) * 128);
                else if (s == 8) c_dma(cs, 0, 2 * k, cb0);
                else if (k == 0) c_dma(cs, 0, 8, cb0);
                MFMA32(aI[1], fa1, fi);
                if (s <= 7) dma_piece(2 * k + 1, ((s + 2) % 3) * 32768, (s + 2) * 128);
                else if (s == 8) c_dma(cs, 0, 2 * k + 1, cb0);
                else if (k == 0) c_dma(cs, 0, 9, cb0);
                SB();
                fa0 = na0; fa1 = na1; fr = nr; fi = ni;
            }
        }
        asm volatile("s_nop 11" : "+v"(aR[0]), "+v"(aI[0]), "+v"(aR[1]), "+v"(aI[1]));
        STAMP(2);
        cpx agg;
        {
            f32x2 sr0 = {0.f, 0.f}, si0 = {0.f, 0.f}, sr1 = {0.f, 0.f}, si1 = {0.f, 0.f};
#pragma unroll
            for (int i = 0; i < 8; ++i) {
                const f32x2 ur = {aR[0][2 * i], aR[0][2 * i + 1]}, ui = {aI[0][2 * i], aI[0][2 * i + 1]};
                const f32x2 qr = Q[15 - i].r.yx, qi = Q[15 - i].i.yx;
                sr0 += qr * ur; si0 += qr * ui; sr0 -= qi * ui; si0 += qi * ur;
                const f32x2 vr = {aR[1][2 * i], aR[1][2 * i + 1]}, vi = {aI[1][2 * i], aI[1][2 * i + 1]};
                const f32x2 pr = Q[7 - i].r.yx, pi = Q[7 - i].i.yx;
                sr1 += pr * vr; si1 += pr * vi; sr1 -= pi * vi; si1 += pi * vr;
            }
            sr0 += sr1; si0 += si1;
            agg = {sr0.x + sr0.y, si0.x + si0.y};
        }
        cpx a0, a1;
        halves(agg.r, a0.r, a1.r); halves(agg.i, a0.i, a1.i);
        const cpx a64 = cfma(l32, a0, a1);
        const unsigned xch = 159744 + (cg * 2) * 256 + ch * 8;
        if (hh == 0) lds_st<f32x2>(xch + th * 256, f32x2{a64.r, a64.i});
        BARRIER();
        cpx pre = {0.f, 0.f};
        if (th) {
            const f32x2 o = lds_ld<f32x2>(xch);
            const cpx o64 = {o.x, o.y};
            const cpx a128 = cfma(l64, o64, a64);
            if (hh == 0 && c < 15)
                __builtin_amdgcn_raw_buffer_store_b128(v4u{__float_as_uint(a128.r), tA, __float_as_uint(a128.i), tB}, ws,
                                                       WS_AGG + (c * 640 + pch) * 16, 0, AUX_SC1);
            pre = hh ? cfma(l32, o64, a0) : o64;
        } else if (hh) pre = a0;
        STAMP(3);
        {
            cpx v = {0.f, 0.f};
#pragma unroll
            for (int t = 0; t < 32; ++t) {
                v = cfma(lam, v, cpx{aR[t >> 4][t & 15], aI[t >> 4][t & 15]});
                const cpx o = cmul(S2, v);
                aR[t >> 4][t & 15] = o.r; aI[t >> 4][t & 15] = o.i;
                SB();
                if (t < 10) c_dma(cs, 1, t, cb1);
                if (t == 10) { if (th && c < 15) WAITV(11); else WAITV(10); }
                if (t >= 10 && t < 20) c_read(cs, cf, 0, t - 10, cb0);
                if (t == 21) asm volatile("s_waitcnt lgkmcnt(0)" ::: "memory");
                if (t >= 21 && t < 31) c_dma(cs, 2, t - 21, cb0);
                SB();
            }
        }
        asm volatile("" : "+v"(aR[0]), "+v"(aI[0]), "+v"(aR[1]), "+v"(aI[1]));
        cpx carry = {0.f, 0.f};
        if (c > 0) {
            v4u ga[4];
            for (int spin = 0; spin < SPIN_CAP; ++spin) {
                MEMFENCE();
                bool ok = true;
#pragma unroll
                for (int i = 0; i < 4; ++i) {
                    const int j = c - 1 - (4 * i + seg);
                    ga[i] = __builtin_amdgcn_raw_buffer_load_b128(ws, WS_AGG + ((j < 0 ? 0 : j) * 640 + pch) * 16, 0, AUX_SC1);
                }
#pragma unroll
                for (int i = 0; i < 4; ++i) ok = ok & ((c - 1 - (4 * i + seg) < 0) | ((ga[i].y == tA) & (ga[i].w == tB)));
                if (__all(ok)) break;
            }
            cpx part = {0.f, 0.f};
#pragma unroll
            for (int i = 3; i >= 0; --i) {
                const bool val = c - 1 - (4 * i + seg) >= 0;
                const cpx a = {val ? __uint_as_float(ga[i].x) : 0.f, val ? __uint_as_float(ga[i].z) : 0.f};
                part = cfma(l512, part, a);
            }
            part = cmul(lgat, part);
            cpx p0, p1;
            halves(part.r, p0.r, p1.r); halves(part.i, p0.i, p1.i);
            const cpx ps = {p0.r + p1.r, p0.i + p1.i};
            if (hh == 0) lds_st<f32x2>(xch + 1024 + th * 256, f32x2{ps.r, ps.i});
            BARRIER();
            const f32x2 o = lds_ld<f32x2>(xch + 1024 + (th ^ 1) * 256);
            carry = {ps.r + o.x, ps.i + o.y};
        }
        STAMP(4);
        const cpx F = cmul(Sl, cfma(lseg, carry, pre));
        if (c == 0) WAITV(0);
        {
            const unsigned hs = w * 8192;
            const unsigned gsw = ((ch >> 1) & 1) << 2 | ((ch >> 3) & 1) << 3 | (ch & 1) | ((ch >> 2) & 1) << 1;
            const unsigned wb = hs + ch * 128;
#pragma unroll
            for (int q = 0; q < 8; ++q) {
                f32x2 hr[2], hi[2];
#pragma unroll
                for (int e = 0; e < 2; ++e) {
                    const int t = 4 * q + 2 * e;
                    const f32x2 ur = {aR[t >> 4][t & 15], aR[t >> 4][(t & 15) + 1]}, ui = {aI[t >> 4][t & 15], aI[t >> 4][(t & 15) + 1]};
                    const cp2 qq = Q[t >> 1];
                    hr[e] = ur + qq.r * F.r - qq.i * F.i;
                    hi[e] = -ui - qq.r * F.i - qq.i * F.r;
                }
                const unsigned a = wb + (((8 * hh + q) ^ gsw) * 8);
                lds_st<v2u>(a, v2u{pk2v(hr[0]), pk2v(hr[1])});
                lds_st<v2u>(a + 4096, v2u{pk2v(hi[0]), pk2v(hi[1])});
            }
            const int gq = lane >> 4, ra = (lane >> 2) & 3, rp = lane & 3;
            unsigned rad[2];
#pragma unroll
            for (int hf = 0; hf < 2; ++hf) {
                const int row = 8 * gq + 4 * hf + ra;
                const unsigned gs = ((row >> 1) & 1) << 2 | ((row >> 3) & 1) << 3 | (row & 1) | ((row >> 2) & 1) << 1;
                rad[hf] = hs + row * 128 + ((rp ^ gs) * 8);
            }
            const unsigned hso = WS_H + ((2 * c + th) * 40 + 4 * g + cg) * 4096;
            v4u hq[8];
#pragma unroll
            for (int i = 0; i < 8; ++i) {
                const v2u x0 = lds_tr((rad[0] ^ ((i & 3) * 32)) + (i >> 2) * 4096), x1 = lds_tr((rad[1] ^ ((i & 3) * 32)) + (i >> 2) * 4096);
                hq[i] = v4u{x0.x, x0.y, x1.x, x1.y};
            }
            SB();
#pragma unroll
            for (int i = 0; i < 8; ++i)
                __builtin_amdgcn_raw_buffer_store_b128(hq[i], ws, lane * 16 + (i & 3) * 1024, hso + (i >> 2) * 8192, AUX_SC1);
            SB();
        }
#pragma unroll
        for (int f = 0; f < 10; ++f) { c_read(cs, cf, 1, f, cb1); c_read(cs, cf, 2, f, cb0); }
        asm volatile("s_waitcnt lgkmcnt(0)" ::: "memory");
#pragma unroll
        for (int i = 0; i < 10; ++i) c_dma(cs, 3, i, cb1);
        asm volatile("s_waitcnt vmcnt(10) lgkmcnt(0)\n\ts_barrier" ::: "memory");
        set_flag(ws, FL_H + c * 10 + g, tA, tB);
        STAMP(5);
        MEMFENCE();
#pragma unroll
        for (int i = 0; i < 10; ++i) c_dma(cs, 4, i, cb0);
        cbuf3 = cb1; cbuf4 = cb0;
    } else if (r < 30) {
        const int jj = r - 20, grp = xl * 10 + jj, H = xl >> 2, tb = 2 * (xl & 3) + H;
        const unsigned stg = w * STG_BYTES, kexp = KEXP_BASE + w * 2 * KEXP_CH;
        KgIn in[2]; v2u ep;
        {
            const rsrc_t r0 = mk(p.sare, SBYTES), r1 = mk(p.saim, SBYTES), r2 = mk(p.sbre, SBYTES), r3 = mk(p.sbim, SBYTES),
                         r4 = mk(p.scre, SBYTES), r5 = mk(p.scim, SBYTES), r6 = mk(p.sldt, VBYTES);
            const unsigned d0 = grp * 8 + 2 * w;
#pragma unroll
            for (int e = 0; e < 2; ++e)
                asm volatile("s_nop 4\n\tbuffer_load_dword %0, %7, %8, %15 offen\n\tbuffer_load_dword %1, %7, %9, %15 offen\n\t"
                             "buffer_load_dword %2, %7, %10, %15 offen\n\tbuffer_load_dword %3, %7, %11, %15 offen\n\t"
                             "buffer_load_dword %4, %7, %12, %15 offen\n\tbuffer_load_dword %5, %7, %13, %15 offen\n\t"
                             "buffer_load_ushort %6, off, %14, %16"
                             : "=&v"(in[e].are), "=&v"(in[e].aim), "=&v"(in[e].bre), "=&v"(in[e].bim), "=&v"(in[e].cre), "=&v"(in[e].cim), "=&v"(in[e].ldt)
                             : "v"(lane * 4), "s"(r0), "s"(r1), "s"(r2), "s"(r3), "s"(r4), "s"(r5), "s"(r6), "s"((d0 + e) * 256), "s"((d0 + e) * 2)
                             : "memory");
            asm volatile("buffer_load_dwordx2 %0, off, %1, 0 sc1" : "=&v"(ep) : "s"(ws) : "memory");
        }
        {
            const int rb = lane >> 3;
#pragma unroll
            for (int i = 0; i < 8; ++i) {
                const unsigned f = (((rb >> 1) & 1) | ((i & 1) << 1)) << 1;
                dma16(kexp + i * 1024, rb * 1280 + (((lane & 7) ^ f) * 16), rx, (256 * tb + 64 * w + 8 * i) * 1280 + jj * 128);
            }
        }
        const rsrc_t rpf = mk(jj < 5 ? p.bre : p.bim, MBYTES);
        const unsigned pfo = (320 * H + 64 * (jj < 5 ? jj : jj - 5)) * 1280;
        touch(lane * 1280, rpf, pfo + w * 128);
        asm volatile("s_waitcnt vmcnt(9)" : "+v"(in[0].are), "+v"(in[0].aim), "+v"(in[0].bre), "+v"(in[0].bim), "+v"(in[0].cre), "+v"(in[0].cim), "+v"(in[0].ldt),
                     "+v"(in[1].are), "+v"(in[1].aim), "+v"(in[1].bre), "+v"(in[1].bim), "+v"(in[1].cre), "+v"(in[1].cim), "+v"(in[1].ldt), "+v"(ep)::"memory");
        tA = rfl(ep.x) + 1; tB = rfl(ep.y) + 1;
        STAMP(1);
        KgState ks = kg_lo(in[0], stg, lane);
        WAITV(1);
        {
            const int g4 = lane >> 4, ra = (lane >> 2) & 3, rp = lane & 3;
            v4u xq[8];
#pragma unroll
            for (int tk = 0; tk < 2; ++tk)
#pragma unroll
                for (int cs = 0; cs < 4; ++cs) {
                    v2u x[2];
#pragma unroll
                    for (int hf = 0; hf < 2; ++hf) {
                        const int row = 32 * tk + 8 * g4 + 4 * hf + ra;
                        const unsigned f = (((row >> 1) & 1) | (((row >> 3) & 1) << 1)) << 1;
                        x[hf] = lds_tr(kexp + row * 128 + (((2 * cs + (rp >> 1)) ^ f) * 16) + (rp & 1) * 8);
                    }
                    xq[tk * 4 + cs] = v4u{x[0].x, x[0].y, x[1].x, x[1].y};
                }
            SB();
            const unsigned vo = ((lane >> 3) & 1) * 32768 + (lane & 7) * 64 + g4 * 16;
#pragma unroll
            for (int i = 0; i < 8; ++i)
                __builtin_amdgcn_raw_buffer_store_b128(xq[i], ws, vo, WS_XT + (jj * 8 + 2 * (i & 3)) * 32768 + (tb * 8 + 2 * w + (i >> 2)) * 512, AUX_SC1);
            SB();
        }
        MEMFENCE();
        touch(lane * 1280, rpf, pfo + (w + 4) * 128);
        touch(lane * 1280, rpf, pfo + (w < 2 ? w + 8 : w + 4) * 128);
        STAMP(2);
        unsigned ga[32];
        {
            const unsigned tabm = stg + TAB_OFF - 2 * 0x4280;
#pragma unroll
            for (int rr = 0; rr < 4; ++rr)
#pragma unroll
                for (int e = 0; e < 4; ++e) {
                    const int l0 = 2044 + (lane & 3) - 32 * (16 * rr + (lane >> 2)) - 8 * e, l1 = l0 - 4;
                    const unsigned pk = pk2((float)l0, (float)l1);
                    unsigned g0 = tabm + 2 * (pk & 0xffffu), g1 = tabm + 2 * (pk >> 16);
                    if (rr == 3) {
                        if (l0 < 128) g0 = stg + TAB_OFF + 2 * l0;
                        if (l1 < 128) g1 = stg + TAB_OFF + 2 * l1;
                    }
                    ga[8 * rr + 2 * e] = g0; ga[8 * rr + 2 * e + 1] = g1;
                }
        }
        kg_hi(ks, stg, lane);
        kg_finish(stg, kexp, lane, ga);
        asm volatile("s_waitcnt vmcnt(2)\n\ts_barrier" ::: "memory");
        set_flag(ws, FL_XT + tb * 10 + jj, tA, tB);
        STAMP(3);
        ks = kg_lo(in[1], stg, lane);
        kg_hi(ks, stg, lane);
        SB();
        poll_flags(ws, WS_FLAG + (FL_XT + (lane & 7) * 10 + (grp >> 3)) * 256, tA, tB);
        v4u uu[8];
#pragma unroll
        for (int i = 0; i < 8; ++i)
            uu[i] = __builtin_amdgcn_raw_buffer_load_b128(ws, (lane >> 3) * 512 + (lane & 7) * 16, WS_XT + grp * 32768 + w * 128 + i * 4096, AUX_SC1);
        SB();
        kg_finish(stg, kexp + KEXP_CH, lane, ga);
        SB();
        {
            const unsigned ua = stg + ((lane >> 2) & 1) * U_CH + 64 + (lane >> 3) * 64 + (lane & 3) * 16;
#pragma unroll
            for (int i = 0; i < 8; ++i) lds_st<v4u>(ua + i * 512, uu[i]);
            if (lane < 8) lds_st<v4u>(stg + (lane >> 2) * U_CH + (lane & 3) * 16, v4u{0, 0, 0, 0});
        }
        STAMP(4);
        MEMFENCE();
        conv_channel<0>(kexp, stg, stg + YST_OFF, lane, cs, cf, stg + TAB_OFF);
        conv_channel<1>(kexp + KEXP_CH, stg + U_CH, stg + YST_OFF + 2, lane, cs, cf, stg + TAB_OFF);
        STAMP(5);
        {
            v4u yq[8];
#pragma unroll
            for (int i = 0; i < 8; ++i) yq[i] = lds_ld<v4u>(stg + YST_OFF + i * 1024 + lane * 16);
            SB();
#pragma unroll
            for (int i = 0; i < 8; ++i)
                __builtin_amdgcn_raw_buffer_store_b128(yq[i], ws, lane * 16, WS_Y + (grp * 4 + w) * 8192 + i * 1024, AUX_SC1);
        }
        asm volatile("s_waitcnt vmcnt(0) lgkmcnt(0)\n\ts_barrier" ::: "memory");
        set_flag(ws, FL_Y + grp, tA, tB);
        cbuf3 = 32768 + w * 10240; cbuf4 = 98304 + w * 10240;
        MEMFENCE();
#pragma unroll
        for (int i = 0; i < 10; ++i) c_dma(cs, 4, i, cbuf4);
    } else {
        const int gc = 2 * (2 * (xl & 3) + 1 - (xl >> 2)) + (r - 30);
        const v2u ep = __builtin_amdgcn_raw_buffer_load_b64(ws, WS_EPOCH, 0, AUX_SC1);
        const rsrc_t rgw = mk(p.gw, VBYTES);
        const unsigned vo = (lane >> 3) * 1280 + (lane & 7) * 16, so = (128 * gc + 32 * w) * 1280;
        float sum[4] = {0.f, 0.f, 0.f, 0.f};
        v4u gv[10], xv[10][4];
#pragma unroll
        for (int s = 0; s < 10; ++s) {
            gv[s] = __builtin_amdgcn_raw_buffer_load_b128(rgw, (lane & 7) * 16, s * 128, 0);
#pragma unroll
            for (int q = 0; q < 4; ++q) xv[s][q] = __builtin_amdgcn_raw_buffer_load_b128(rx, vo, so + q * 8 * 1280 + s * 128, 0);
        }
        SB();
#pragma unroll
        for (int s = 0; s < 10; ++s)
#pragma unroll
            for (int e = 0; e < 4; ++e) {
                const f32x2 gw2 = {bflo(gv[s][e]), bfhi(gv[s][e])};
#pragma unroll
                for (int q = 0; q < 4; ++q) {
                    const unsigned pr = pk2v(f32x2{bflo(xv[s][q][e]), bfhi(xv[s][q][e])} * gw2);
                    sum[q] += bflo(pr); sum[q] += bfhi(pr);
                }
            }
        tA = rfl(ep.x) + 1; tB = rfl(ep.y) + 1;
#pragma unroll
        for (int q = 0; q < 4; ++q) {
            float v = sum[q];
            v += __shfl_xor(v, 1); v += __shfl_xor(v, 2); v += __shfl_xor(v, 4);
            const float z = rbf(v), gt = rbf(1.f / (1.f + expf(-z)));
            if ((lane & 7) == 0)
                __builtin_amdgcn_raw_buffer_store_b32(__float_as_uint(gt), ws, WS_G + (128 * gc + 32 * w + 8 * q + (lane >> 3)) * 4, 0, AUX_SC1);
        }
        asm volatile("s_waitcnt vmcnt(0) lgkmcnt(0)\n\ts_barrier" ::: "memory");
        set_flag(ws, FL_G + gc, tA, tB);
        STAMP(5);
        MEMFENCE();
        cbuf3 = 32768 + w * 10240; cbuf4 = 98304 + w * 10240;
#pragma unroll
        for (int pb = 0; pb < 3; ++pb) {
#pragma unroll
            for (int i = 0; i < 10; ++i) c_dma(cs, pb, i, cbuf3);
            WAITV(0);
#pragma unroll
            for (int f = 0; f < 10; ++f) c_read(cs, cf, pb, f, cbuf3);
            asm volatile("s_waitcnt lgkmcnt(0)" ::: "memory");
        }
#pragma unroll
        for (int i = 0; i < 10; ++i) { c_dma(cs, 3, i, cbuf3); c_dma(cs, 4, i, cbuf4); }
    }

    const int own = (w + ct) & 3;
    const int tok = 64 * tt + 16 * own + (lane & 15), cq = lane >> 4;
    v2u xe[5], da[5], db[5];
    {
        const rsrc_t ra = mk(p.dska, VBYTES), rb = mk(p.dskb, VBYTES);
#pragma unroll
        for (int nt = 0; nt < 5; ++nt) {
            xe[nt] = __builtin_amdgcn_raw_buffer_load_b64(rx, tok * 1280 + cq * 8, ct * 160 + nt * 32, 0);
            da[nt] = __builtin_amdgcn_raw_buffer_load_b64(ra, cq * 8, ct * 160 + nt * 32, 0);
            db[nt] = __builtin_amdgcn_raw_buffer_load_b64(rb, cq * 8, ct * 160 + nt * 32, 0);
        }
    }
    {
        const int g0 = (10 * w) >> 2;
        poll_flags(ws, WS_FLAG + (lane < 3 ? FL_H + (tt >> 1) * 10 + g0 + lane : FL_G + (tt >> 1)) * 256, tA, tB);
    }
    STAMP(6);
    if (r < 20 || r >= 30) {
#pragma unroll
        for (int f = 0; f < 10; ++f) c_read(cs, cf, 3, f, cbuf3);
    }
#pragma unroll
    for (int f = 0; f < 10; ++f) c_read(cs, cf, 4, f, cbuf4);
    const float gate = __uint_as_float(__builtin_amdgcn_raw_buffer_load_b32(ws, WS_G + tok * 4, 0, AUX_SC1));
    f32x4 fin[5];
    {
        constexpr int WIN = 20;
        v4u hv[WIN];
        const unsigned hb = WS_H + (tt * 40 + 10 * w) * 4096;
        unsigned so[4];
#pragma unroll
        for (int o = 0; o < 4; ++o) so[o] = hb + ((own + 1 + o) & 3) * 1024;
#pragma unroll
        for (int i = 0; i < WIN; ++i) hv[i] = __builtin_amdgcn_raw_buffer_load_b128(ws, lane * 16, so[i / 10] + (i % 10) * 4096, AUX_SC1);
        SB();
#pragma unroll
        for (int o = 0; o < 4; ++o) {
            f32x4 acc[5];
#pragma unroll
            for (int j = 0; j < 10; ++j) {
                const int i = 10 * o + j;
                const v4u hf = hv[i % WIN];
#pragma unroll
                for (int nt = 0; nt < 5; ++nt) {
                    if (j == 0) MFMA16AZ(acc[nt], cf[nt], hf); else MFMA16A(acc[nt], cf[5 * j + nt], hf);
                }
                if (i + WIN < 40)
                    hv[i % WIN] = __builtin_amdgcn_raw_buffer_load_b128(ws, lane * 16, so[(i + WIN) / 10] + ((i + WIN) % 10) * 4096, AUX_SC1);
                SB();
            }
            asm volatile("s_nop 7" : "+v"(acc[0]), "+v"(acc[1]), "+v"(acc[2]), "+v"(acc[3]), "+v"(acc[4]));
            if (o < 3) {
                const unsigned ra = red_off(((own + 1 + o) & 3) * 3 + o) + lane * 16;
#pragma unroll
                for (int nt = 0; nt < 5; ++nt) lds_st<f32x4>(ra + nt * 1024, acc[nt]);
            } else {
#pragma unroll
                for (int nt = 0; nt < 5; ++nt) fin[nt] = acc[nt];
            }
            SB();
        }
    }
    STAMP(7);
    poll_flags(ws, WS_FLAG + (FL_Y + ct * 10 + (lane < 10 ? lane : 0)) * 256, tA, tB);
    unsigned yv[5][2];
#pragma unroll
    for (int nt = 0; nt < 5; ++nt)
#pragma unroll
        for (int e = 0; e < 2; ++e)
            yv[nt][e] = __builtin_amdgcn_raw_buffer_load_b32(ws, tok * 4 + cq * 16384, WS_Y + (40 * ct + 8 * nt + e) * 8192, AUX_SC1);
    BARRIER();
#pragma unroll
    for (int o = 0; o < 3; ++o)
#pragma unroll
        for (int nt = 0; nt < 5; ++nt) fin[nt] += lds_ld<f32x4>(red_off(own * 3 + o) + lane * 16 + nt * 1024);
    STAMP(8);
    {
        const rsrc_t ro = mk(p.out, SEQ * DM * 4);
        const float omg = rbf(1.f - gate);
        f32x4 oo[5];
#pragma unroll
        for (int nt = 0; nt < 5; ++nt) {
            f32x4 o;
#pragma unroll
            for (int e = 0; e < 2; ++e) {
                const f32x2 x2 = {bflo(xe[nt][e]), bfhi(xe[nt][e])};
                const unsigned sa = pk2v(x2 * f32x2{bflo(da[nt][e]), bfhi(da[nt][e])});
                const unsigned sb = pk2v(x2 * f32x2{bflo(db[nt][e]), bfhi(db[nt][e])});
                const unsigned bb = pk2v(f32x2{bflo(yv[nt][e]), bfhi(yv[nt][e])} + f32x2{bflo(sb), bfhi(sb)});
                const unsigned ob = pk2v(f32x2{bflo(bb), bfhi(bb)} * omg);
                const float a0 = fin[nt][2 * e] + bflo(sa), a1 = fin[nt][2 * e + 1] + bfhi(sa);
                o[2 * e] = __fadd_rn(__fmul_rn(gate, a0), bflo(ob));
                o[2 * e + 1] = __fadd_rn(__fmul_rn(gate, a1), bfhi(ob));
            }
            oo[nt] = o;
        }
        SB();
#pragma unroll
        for (int nt = 0; nt < 5; ++nt)
            __builtin_amdgcn_raw_buffer_store_b128(__builtin_bit_cast(v4u, oo[nt]), ro, tok * 2560 + cq * 16, ct * 320 + nt * 64, 0);
        asm volatile("s_nop 1");
        SB();
    }
    if (b == 255 && w == 0) {
        for (int k = 0; k < 4; ++k) {
            const int i = k * 64 + lane;
            poll_flags(ws, WS_FLAG + (i < 240 ? i : i + 80) * 256, tA, tB);
        }
        if (lane == 0) __builtin_amdgcn_raw_buffer_store_b64(v2u{tA, tB}, ws, WS_EPOCH, 0, AUX_SC1);
    }
    asm volatile("s_waitcnt vmcnt(0)" ::: "memory");
    STAMP(9);
    if (tid < 16) ((GAS v4u*)((char*)p.ws + WS_STAMP))[blockIdx.x * 16 + tid] = lds_ld<v4u>(163328 + tid * 16);
    if (b == RWG && w == 0) {
        const v4u s0 = lds_ld<v4u>(163328 + RFROM * 16), s1 = lds_ld<v4u>(163328 + RTO * 16);
        unsigned d = RCYC ? (rfl(s1.z) - rfl(s0.z)) / 10 : (rfl(s1.x) - rfl(s0.x)) * 10;
        if (d > 100000u) d = 100000u;
        const u64 t0 = __builtin_amdgcn_s_memrealtime();
        while (__builtin_amdgcn_s_memrealtime() - t0 < d) __builtin_amdgcn_s_sleep(2);
    }
}

extern "C" void kernel_launch(void* const* d_in, const int* in_sizes, int n_in, void* d_out, int out_size, void* d_ws,
                              size_t ws_size, hipStream_t stream) {
    Params p;
    p.x = d_in[0]; p.lre = d_in[1]; p.lim = d_in[2]; p.bre = d_in[3]; p.bim = d_in[4]; p.cre = d_in[5]; p.cim = d_in[6];
    p.ldt = d_in[7]; p.dska = d_in[8];
    p.sare = d_in[10]; p.saim = d_in[11]; p.sbre = d_in[12]; p.sbim = d_in[13]; p.scre = d_in[14]; p.scim = d_in[15];
    p.sldt = d_in[16]; p.dskb = d_in[17]; p.gw = d_in[18];
    p.out = d_out; p.ws = d_ws;
    (void)hipFuncSetAttribute((const void*)k_fused, hipFuncAttributeMaxDynamicSharedMemorySize, LDS_BYTES);
    k_fused<<<256, 256, LDS_BYTES, stream>>>(p);
}
```

```cpp
#define DIAG 1
#define RWG 27
#define RFROM 0
#define RTO 0
#define RCYC 0
#define HWIN 6
#include <hip/hip_runtime.h>
#include <stdint.h>

#define DI __device__ __forceinline__
#define GAS __attribute__((address_space(1)))
#define LAS __attribute__((address_space(3)))
typedef unsigned short u16;
typedef unsigned long long u64;
typedef unsigned v4u __attribute__((ext_vector_type(4)));
typedef unsigned v2u __attribute__((ext_vector_type(2)));
typedef float f32x2 __attribute__((ext_vector_type(2)));
typedef float f32x4 __attribute__((ext_vector_type(4)));
typedef float f32x16 __attribute__((ext_vector_type(16)));
typedef short s16x4 __attribute__((ext_vector_type(4)));
typedef __bf16 bf16x2 __attribute__((ext_vector_type(2)));
typedef __amdgpu_buffer_rsrc_t rsrc_t;

constexpr int SEQ = 2048, DM = 640, NMODE = 128;
constexpr unsigned XBYTES = SEQ * DM * 2, MBYTES = DM * DM * 2, SBYTES = DM * NMODE * 2, VBYTES = DM * 2;

constexpr unsigned WS_EPOCH = 0;
constexpr unsigned WS_FLAG = 4096;
constexpr unsigned FL_H = 0, FL_XT = 160, FL_Y = 240, FL_G = 320, FL_N = 336;
constexpr unsigned WS_G = 96 * 1024;
constexpr unsigned WS_AGG = 128 * 1024;
constexpr unsigned WS_H = 512 * 1024;
constexpr unsigned WS_XT = WS_H + SEQ * 1280 * 2;
constexpr unsigned WS_Y = WS_XT + XBYTES;
constexpr unsigned WS_END = WS_Y + XBYTES;
constexpr unsigned WS_STAMP = 16u << 20;

constexpr int LDS_BYTES = 163840;
constexpr unsigned LDS_DUMMY = LDS_BYTES - 256;
constexpr int SPIN_CAP = 20000;
constexpr int AUX_SC1 = 16;

#define RL 1
#define HPF 0
#define APF 1
#define BPF 1
#define PFNT 0
#define TOUCH touch
#define DMAX dma16
#define NT_STAGES 0
#define NC1 20
#define NC2 10

struct Params {
    const void *x, *lre, *lim, *bre, *bim, *cre, *cim, *ldt, *dska;
    const void *sare, *saim, *sbre, *sbim, *scre, *scim, *sldt, *dskb, *gw;
    void *out, *ws;
};

DI rsrc_t mk(const void* p, unsigned bytes) { return __builtin_amdgcn_make_buffer_rsrc((void*)p, (short)0, (int)bytes, 0x00020000); }
DI float bflo(unsigned v) { return __uint_as_float(v << 16); }
DI float bfhi(unsigned v) { return __uint_as_float(v & 0xffff0000u); }
DI unsigned pk2(float a, float b) { f32x2 v = {a, b}; return __builtin_bit_cast(unsigned, __builtin_convertvector(v, bf16x2)); }
DI unsigned pk2v(f32x2 v) { return __builtin_bit_cast(unsigned, __builtin_convertvector(v, bf16x2)); }
DI float rbf(float a) { return bflo(pk2(a, a)); }
DI unsigned rfl(unsigned v) { return __builtin_amdgcn_readfirstlane(v); }
template <int I> struct IC { static constexpr int v = I; };
template <int B, int E, class F> DI void sfor(F&& f) { if constexpr (B < E) { f(IC<B>{}); sfor<B + 1, E>(f); } }
#define SB() __builtin_amdgcn_sched_barrier(0)
#define MEMFENCE() asm volatile("" ::: "memory")

template <class T> struct Any { typedef T __attribute__((may_alias)) type; };
template <class T> DI T lds_ld(unsigned a) { return *(const LAS typename Any<T>::type*)(uintptr_t)a; }
template <class T> DI void lds_st(unsigned a, T v) { *(LAS typename Any<T>::type*)(uintptr_t)a = v; }
DI v2u lds_tr(unsigned a) { return __builtin_bit_cast(v2u, __builtin_amdgcn_ds_read_tr16_b64_v4i16((LAS s16x4*)(uintptr_t)a)); }

#define MFMA32(acc, a, b) asm volatile("v_mfma_f32_32x32x16_bf16 %0, %1, %2, %0" : "+v"(acc) : "v"(a), "v"(b))
#define MFMA32Z(acc, a, b) asm volatile("v_mfma_f32_32x32x16_bf16 %0, %1, %2, 0" : "=&v"(acc) : "v"(a), "v"(b))
template <bool Z> DI f32x16 mfma32(v4u a, v4u b, f32x16 c) {
    if constexpr (Z) asm volatile("v_mfma_f32_32x32x16_bf16 %0, %1, %2, 0" : "=&v"(c) : "v"(a), "v"(b));
    else asm volatile("v_mfma_f32_32x32x16_bf16 %0, %1, %2, %0" : "+v"(c) : "v"(a), "v"(b));
    return c;
}
DI f32x4 mfma16(v4u a, v4u b, f32x4 c) { asm volatile("v_mfma_f32_16x16x32_bf16 %0, %1, %2, %0" : "+v"(c) : "a"(a), "v"(b)); return c; }
DI f32x4 mfma16z(v4u a, v4u b) { f32x4 c; asm volatile("v_mfma_f32_16x16x32_bf16 %0, %1, %2, 0" : "=&v"(c) : "a"(a), "v"(b)); return c; }
#define MFMA16A(acc, a, b) asm volatile("v_mfma_f32_16x16x32_bf16 %0, %1, %2, %0" : "+v"(acc) : "a"(a), "v"(b))
#define MFMA16AZ(acc, a, b) asm volatile("v_mfma_f32_16x16x32_bf16 %0, %1, %2, 0" : "=&v"(acc) : "a"(a), "v"(b))

DI void dma16(unsigned ldsoff, unsigned voff, rsrc_t rs, unsigned soff) {
    asm volatile("s_mov_b32 m0, %0\n\ts_nop 0\n\tbuffer_load_dwordx4 %1, %2, %3 offen lds" ::"s"(ldsoff), "v"(voff), "s"(rs), "s"(soff) : "memory");
}
DI void dma16nt(unsigned ldsoff, unsigned voff, rsrc_t rs, unsigned soff) {
    asm volatile("s_mov_b32 m0, %0\n\ts_nop 0\n\tbuffer_load_dwordx4 %1, %2, %3 offen nt lds" ::"s"(ldsoff), "v"(voff), "s"(rs), "s"(soff) : "memory");
}
DI void touchnt(unsigned voff, rsrc_t rs, unsigned soff) {
    asm volatile("s_mov_b32 m0, %0\n\ts_nop 4\n\tbuffer_load_dword %1, %2, %3 offen nt lds" ::"s"(LDS_DUMMY), "v"(voff), "s"(rs), "s"(soff) : "memory");
}
DI void touch(unsigned voff, rsrc_t rs, unsigned soff) {
    asm volatile("s_mov_b32 m0, %0\n\ts_nop 4\n\tbuffer_load_dword %1, %2, %3 offen lds" ::"s"(LDS_DUMMY), "v"(voff), "s"(rs), "s"(soff) : "memory");
}
#define CODE_MARK(x) asm volatile(".Lkf_end_" #x ":")
#define WAITV(n) asm volatile("s_waitcnt vmcnt(%0)" ::"n"(n) : "memory")
#define BARRIER() asm volatile("s_waitcnt lgkmcnt(0)\n\ts_barrier" ::: "memory")

#define STAMP(i) do { SB(); if (((i) == RFROM || (i) == RTO) && w == 0) { u64 t_, c_; asm volatile("s_memrealtime %0\n\ts_memtime %1\n\ts_waitcnt lgkmcnt(0)" : "=s"(t_), "=s"(c_)::"memory"); \
    lds_st<v4u>(163328 + (i) * 16, v4u{(unsigned)t_, (unsigned)(t_ >> 32), (unsigned)c_, (unsigned)(c_ >> 32)}); asm volatile("s_waitcnt lgkmcnt(0)" ::: "memory"); } SB(); } while (0)

DI void poll_flags(rsrc_t ws, unsigned off, unsigned tA, unsigned tB) {
    for (int spin = 0; spin < SPIN_CAP; ++spin) {
        MEMFENCE();
        const v2u f = __builtin_amdgcn_raw_buffer_load_b64(ws, off, 0, AUX_SC1);
        if (__all((f.x == tA) & (f.y == tB))) break;
    }
    __builtin_amdgcn_fence(__ATOMIC_ACQUIRE, "wavefront");
    MEMFENCE();
}
DI void set_flag(rsrc_t ws, unsigned idx, unsigned tA, unsigned tB) {
    if (threadIdx.x == 0) __builtin_amdgcn_raw_buffer_store_b64(v2u{tA, tB}, ws, WS_FLAG + idx * 256, 0, AUX_SC1);
}

#define PIN1(a) asm volatile("" : "+v"(a))
#define PIN2(a, b) asm volatile("" : "+v"(a), "+v"(b))
#define CMUL_A(dr, di, ar, br, bi) asm volatile("v_mul_f32 %0, %2, %3\n\tv_mul_f32 %1, %2, %4" : "=&v"(dr), "=&v"(di) : "v"(ar), "v"(br), "v"(bi))
#define CMUL_B(dr, di, ai, br, bi) asm volatile("v_fma_f32 %0, -%2, %4, %0\n\tv_fmac_f32 %1, %2, %3" : "+v"(dr), "+v"(di) : "v"(ai), "v"(br), "v"(bi))
struct cpx { float r, i; };
DI cpx cmul(cpx a, cpx b) {
    cpx d;
    asm("v_mul_f32 %0, %2, %4\n\tv_mul_f32 %1, %2, %5\n\tv_fma_f32 %0, -%3, %5, %0\n\tv_fmac_f32 %1, %3, %4"
        : "=&v"(d.r), "=&v"(d.i) : "v"(a.r), "v"(a.i), "v"(b.r), "v"(b.i));
    return d;
}
DI cpx cmulv(cpx a, cpx b) {
    cpx d;
    asm volatile("v_mul_f32 %0, %2, %4\n\tv_mul_f32 %1, %2, %5\n\tv_fma_f32 %0, -%3, %5, %0\n\tv_fmac_f32 %1, %3, %4"
                 : "=&v"(d.r), "=&v"(d.i) : "v"(a.r), "v"(a.i), "v"(b.r), "v"(b.i));
    return d;
}
DI cpx cfma(cpx a, cpx b, cpx c) {
    cpx d;
    asm("v_fma_f32 %0, %2, %4, %6\n\tv_fma_f32 %1, %2, %5, %7\n\tv_fma_f32 %0, -%3, %5, %0\n\tv_fmac_f32 %1, %3, %4"
        : "=&v"(d.r), "=&v"(d.i) : "v"(a.r), "v"(a.i), "v"(b.r), "v"(b.i), "v"(c.r), "v"(c.i));
    return d;
}
struct cp2 { f32x2 r, i; };
DI cp2 cmul2(cp2 a, cp2 b) { return {a.r * b.r - a.i * b.i, a.r * b.i + a.i * b.r}; }
DI cp2 cmul2s(cp2 a, cpx b) { return {a.r * b.r - a.i * b.i, a.r * b.i + a.i * b.r}; }
DI void halves(float v, float& lo, float& hi) {
    auto s = __builtin_amdgcn_permlane32_swap(__float_as_uint(v), __float_as_uint(v), false, false);
    lo = __uint_as_float(s[0]); hi = __uint_as_float(s[1]);
}

struct CStage { rsrc_t r0, r1; unsigned vo[2], ra[2]; int w; };
DI void c_dma(const CStage& c, int pb, int i, unsigned buf) {
    const unsigned u = 5u * c.w + pb;
    dma16(buf + i * 1024, c.vo[i & 1], (pb & 1) ? c.r1 : c.r0, (u >> 1) * 128u + i * 8u * 1280u);
}
DI void c_read(const CStage& c, v4u (&cf)[40], int pb, int f, unsigned buf) {
    cf[8 * pb + f] = lds_ld<v4u>(buf + c.ra[f >> 2] + (f & 3) * 2048);
}

struct KgIn { unsigned are, aim, bre, bim, cre, cim, ldt; };
DI cp2 cexp2(f32x2 zr2, f32x2 zi2, float l) {
    const f32x2 a = zr2 * l, b = zi2 * l;
    cp2 o;
    const float m0 = __builtin_amdgcn_exp2f(a.x), m1 = __builtin_amdgcn_exp2f(a.y);
    const float f0 = __builtin_amdgcn_fractf(b.x), f1 = __builtin_amdgcn_fractf(b.y);
    o.r = f32x2{m0 * __builtin_amdgcn_cosf(f0), m1 * __builtin_amdgcn_cosf(f1)};
    o.i = f32x2{m0 * __builtin_amdgcn_sinf(f0), m1 * __builtin_amdgcn_sinf(f1)};
    return o;
}
constexpr unsigned BW_STRIDE = 40720, KEXP_RHO = 1216, KEXP_CH = 4 * KEXP_RHO, U_CH = 4160;
constexpr unsigned BW_K1 = 0, BW_U = KEXP_CH, BW_STG = BW_U + 2 * U_CH, STG_CH = 53 * 16, BW_END = BW_STG + 32 * STG_CH;
constexpr unsigned BW_K2 = BW_END - KEXP_CH, BW_C0 = BW_STG, BW_C1 = BW_C0 + 10240, BW_TAB = BW_C1 + 10240, BW_Y = 0;
static_assert(BW_END <= BW_STRIDE && BW_TAB + 1536 <= BW_K2, "role B LDS map");
DI void put_row(unsigned a, int row, cp2 v) { lds_st<v2u>(a + row * 16, v2u{pk2v(v.r), pk2v(v.i)}); }
DI cp2 csqr2(cp2 a) { const f32x2 t = a.r * a.i; return {a.r * a.r - a.i * a.i, t + t}; }

struct KgState { cp2 w, e1, e2, e4, e256; };
template <class F>
DI KgState kg_lo(const KgIn& in, unsigned sa, F&& mid) {
    const float dt = rbf(__expf(bflo(in.ldt)));
    KgState s;
    const f32x2 zr = f32x2{bflo(in.are), bfhi(in.are)} * dt, zi = f32x2{bflo(in.aim), bfhi(in.aim)} * dt;
    const f32x2 zr2 = zr * 1.44269504088896341f, zi2 = zi * 0.15915494309189535f;
    const cp2 e8 = cexp2(zr2, zi2, 8.f);
    cp2 v0 = {f32x2{1.f, 1.f}, f32x2{0.f, 0.f}}, v1 = e8, v2 = csqr2(e8), v3 = cmul2(v2, e8);
    const cp2 e32 = csqr2(v2);
    put_row(sa, 21, v0); put_row(sa, 22, v1); put_row(sa, 23, v2); put_row(sa, 24, v3);
    v0 = e32;
    SB(); mid(IC<0>{}); SB();
    sfor<1, 8>([&](auto K) {
        constexpr int k = K.v;
        if constexpr (k > 1) v0 = cmul2(v0, e32);
        v1 = cmul2(v1, e32); v2 = cmul2(v2, e32); v3 = cmul2(v3, e32);
        put_row(sa, 21 + 4 * k, v0); put_row(sa, 22 + 4 * k, v1); put_row(sa, 23 + 4 * k, v2); put_row(sa, 24 + 4 * k, v3);
        SB(); mid(K); SB();
    });
    s.e256 = cmul2(v0, e32); s.e256.i = -s.e256.i;
    const cp2 db = {f32x2{bflo(in.bre), bfhi(in.bre)} * dt, f32x2{bflo(in.bim), bfhi(in.bim)} * dt};
    const cp2 cc = {f32x2{bflo(in.cre), bfhi(in.cre)}, f32x2{bflo(in.cim), bfhi(in.cim)}};
    s.w = cmul2(cc, db); s.w.i = -s.w.i;
    s.e1 = cexp2(zr2, -zi2, 1.f); s.e2 = csqr2(s.e1); s.e4 = csqr2(s.e2);
    return s;
}
constexpr int HSRC[21] = {0, 0, 0, 1, 0, 1, 2, 3, 0, 8, 8, 9, 8, 12, 12, 14, 14, 16, 17, 18, 19};
constexpr int HMUL[21] = {0, 0, 1, 1, 2, 2, 2, 2, 3, 1, 2, 2, 3, 2, 3, 2, 3, 3, 3, 3, 3};
DI const cp2& hmul(const KgState& s, int k) { return k == 0 ? s.e1 : k == 1 ? s.e2 : k == 2 ? s.e4 : s.e256; }
template <class F>
DI void kg_hi(const KgState& s, unsigned sa, F&& mid) {
    cp2 v[21];
    v[0] = s.w;
    sfor<0, 21>([&](auto R) {
        constexpr int r = R.v;
        if constexpr (r > 0) v[r] = cmul2(v[HSRC[r]], hmul(s, HMUL[r]));
        if constexpr (r & 1) { put_row(sa, r - 1, v[r - 1]); put_row(sa, r, v[r]); SB(); mid(R); SB(); }
        if constexpr (r == 20) put_row(sa, r, v[r]);
    });
}
struct KgAddr { unsigned ra, t0, t1, t2, t3, tb, wa; unsigned lag[16]; };
DI unsigned tab_addr(const KgAddr& k, int n) {
    unsigned d;
    if (n & 1) asm("v_mad_u32_u16 %0, %1, 2, %2 op_sel:[1,0,0,0]" : "=v"(d) : "v"(k.lag[n >> 1]), "v"(k.tb));
    else asm("v_mad_u32_u16 %0, %1, 2, %2" : "=v"(d) : "v"(k.lag[n >> 1]), "v"(k.tb));
    return d;
}
DI void kg_scatter(const KgAddr& k, const f32x16& d, int part) {
    if (part == 0) lds_st<v2u>(k.t0, v2u{pk2(d[0], d[1]), pk2(d[2], d[3])});
    if (part == 1) { lds_st<unsigned>(k.t1, pk2(d[4], d[5])); lds_st<unsigned>(k.t2, pk2(d[6], d[7])); }
    if (part == 2) { const unsigned p = pk2(d[8], d[9]); lds_st<u16>(k.t3, (u16)p); lds_st<u16>(k.t3 + 64, (u16)(p >> 16)); }
    if (part == 3) { const unsigned p = pk2(d[10], d[11]); lds_st<u16>(k.t3 + 128, (u16)p); lds_st<u16>(k.t3 + 192, (u16)(p >> 16)); }
}
DI void kg_finish(const KgAddr& k, unsigned koff, int lane) {
    f32x16 d;
    v4u a = lds_ld<v4u>(k.ra), b = lds_ld<v4u>(k.ra + 21 * 16);
#pragma unroll
    for (int ks = 0; ks < 16; ++ks) {
        v4u na = a, nb = b;
        if (ks < 15) { na = lds_ld<v4u>(k.ra + (ks + 1) * 2 * STG_CH); nb = lds_ld<v4u>(k.ra + (ks + 1) * 2 * STG_CH + 21 * 16); }
        SB();
        if (ks == 0) MFMA32Z(d, a, b); else MFMA32(d, a, b);
        SB();
        a = na; b = nb;
    }
    asm volatile("s_nop 11" : "+v"(d));
#pragma unroll
    for (int q = 0; q < 4; ++q) kg_scatter(k, d, q);
    unsigned t[32];
#pragma unroll
    for (int n = 0; n < 32; ++n) t[n] = lds_ld<u16>(tab_addr(k, n));
    SB();
#pragma unroll
    for (int r = 0; r < 4; ++r)
        lds_st<v4u>(k.wa + koff + r * 256, v4u{t[8 * r] | t[8 * r + 1] << 16, t[8 * r + 2] | t[8 * r + 3] << 16, t[8 * r + 4] | t[8 * r + 5] << 16, t[8 * r + 6] | t[8 * r + 7] << 16});
    if (lane < 28) lds_st<v4u>(k.wa + koff + 1024, v4u{0, 0, 0, 0});
}

DI float shadd(float prev, float cur, float y) {
    float d;
    asm volatile("v_add_f32_dpp %0, %1, %3 wave_ror:1 row_mask:0xf bank_mask:0xf\n\t"
                 "v_add_f32_dpp %0, %2, %3 wave_shr:1 row_mask:0xf bank_mask:0xf" : "=&v"(d) : "v"(prev), "v"(cur), "v"(y));
    return d;
}
DI float shadd0(float cur, float y) {
    float d;
    asm volatile("v_add_f32_dpp %0, %1, %2 wave_shr:1 row_mask:0xf bank_mask:0xf bound_ctrl:0" : "=&v"(d) : "v"(cur), "v"(y));
    return d;
}

constexpr int epi_upto(int i, int dl) {
    int e = 0;
    for (int a = 1; a <= i; ++a)
        for (int b = 0; b < 8 - a && (a < i || b <= dl); ++b) {
            const int lim = 12 * (a <= 4 ? a : a - 1);
            const int n = (b == 7 - a) ? 1 : 2;
            e = e + n > lim ? lim : e + n;
        }
    return e;
}
template <bool DRAIN, class F>
DI void conv_channel(unsigned kexp, unsigned ub, int lane, float (&y)[32], F&& fill) {
    const int hh = lane >> 5;
    const unsigned aA = kexp + (lane & 3) * KEXP_RHO + (56 - 8 * ((lane & 31) >> 2) + 8 * hh) * 2;
    const unsigned aB = ub + 2 * (4 + 32 * hh + 4 * ((lane >> 2) & 3) + 16 * ((lane >> 4) & 1) + 4 * (lane & 3));
    f32x16 acc[8];
    v4u af[2][8];
    float p2[32], p1[32];
    auto epi = [&](auto E) {
        constexpr int e = E.v, G = 4 * (e / 12) + (e & 3), st = (e % 12) >> 2;
        const float yy = acc[G >> 2][4 * (G & 3) + 2 - st];
        if constexpr (st == 0) {
            const float cur = acc[G >> 2][4 * (G & 3) + 3];
            if constexpr (G == 0) p2[0] = shadd0(cur, yy);
            else { const float prev = acc[(G - 1) >> 2][4 * ((G - 1) & 3) + 3]; p2[G] = shadd(prev, cur, yy); }
        } else if constexpr (st == 1) {
            if constexpr (G == 0) p1[0] = shadd0(p2[0], yy); else p1[G] = shadd(p2[G - 1], p2[G], yy);
        } else {
            if constexpr (G == 0) y[0] = shadd0(p1[0], yy); else y[G] = shadd(p1[G - 1], p1[G], yy);
        }
    };
#pragma unroll
    for (int dl = 0; dl < 8; ++dl) af[0][dl] = lds_ld<v4u>(aA + (64 * (7 - dl)) * 2);
    v2u b0 = lds_tr(aB), b1 = lds_tr(aB + 32), b2 = lds_tr(aB + 512), b3 = lds_tr(aB + 512 + 32);
    sfor<0, 32>([&](auto CI) {
        constexpr int c = CI.v >> 3, i = CI.v & 7;
        const v4u bf = {b0.x, b0.y, b1.x, b1.y};
        b0 = b2; b1 = b3;
        constexpr int ni = (i + 2) & 7, nc = c + (i >= 6);
        if constexpr (DRAIN && c + i > 0 && i < 6) asm volatile("s_waitcnt lgkmcnt(0)" ::: "memory");
        if constexpr (nc < 4) { b2 = lds_tr(aB + 128 * (4 * ni + nc)); b3 = lds_tr(aB + 128 * (4 * ni + nc) + 32); }
        if constexpr (c < 3) af[(c + 1) & 1][i] = lds_ld<v4u>(aA + (64 * (7 - i) + 16 * (c + 1)) * 2);
        SB();
        sfor<0, 8 - i>([&](auto DL) {
            constexpr int dl = DL.v, m = 36 * c + 8 * i - i * (i - 1) / 2 + dl;
            if constexpr (c == 0 && i == 0) MFMA32Z(acc[dl], af[0][dl], bf);
            else MFMA32(acc[i + dl], af[c & 1][dl], bf);
            SB();
            fill(IC<m>{}, IC<(dl == 7 - i) ? -1 : m - CI.v>{});
            if constexpr (c == 3 && i > 0) sfor<(dl ? epi_upto(i, dl - 1) : epi_upto(i - 1, 8 - i)), epi_upto(i, dl)>(epi);
            SB();
        });
    });
    asm volatile("s_nop 7" : "+v"(acc[5]), "+v"(acc[6]), "+v"(acc[7]));
    sfor<epi_upto(7, 0), 96>(epi);
}

__global__ __launch_bounds__(256, 1) void k_fused(Params p) {
    const int tid = threadIdx.x, lane = tid & 63;
    const int w = __builtin_amdgcn_readfirstlane(tid >> 6);
    const int b = blockIdx.x, xl = b & 7, r = b >> 3;
    if (tid < 16) lds_st<v4u>(163328 + tid * 16, v4u{0, 0, 0, 0});
    STAMP(0);
    if (w == 3) {
        u64 pc; unsigned len;
        asm volatile("s_getpc_b64 %0\n.Lkf_pc:\n\ts_mov_b32 %1, .Lkf_end_a-.Lkf_pc\n\ts_max_i32 %1, %1, .Lkf_end_b-.Lkf_pc\n\t"
                     "s_max_i32 %1, %1, .Lkf_end_g-.Lkf_pc" : "=s"(pc), "=&s"(len)::"scc");
        touch((r * 20 + lane) * 128 + (lane < 20 ? 0 : 0x40000000), mk((const void*)pc, len), 0);
    }
    const rsrc_t ws = mk(p.ws, WS_END);
    const rsrc_t rx = mk(p.x, XBYTES);
    unsigned tA, tB;
    constexpr unsigned BW_PAR = BW_U + U_CH;
    constexpr unsigned A_PAR = 131072;
    const int c = 4 * (xl & 3) + (r & 3), g = 5 * (xl >> 2) + (r >> 2);
    const rsrc_t rbre = mk(p.bre, MBYTES), rbim = mk(p.bim, MBYTES);
    const unsigned gch = ((lane & 7) ^ ((4 * w + (lane >> 4)) & 7)) * 16;
    const unsigned vox = (32 * (lane >> 5) + ((lane >> 3) & 3)) * 1280 + gch, vob = (lane >> 3) * 1280 + gch;
    const unsigned sox = (128 * c + 4 * w) * 1280, sob = (64 * g + 8 * w) * 1280;
    auto dma_piece = [&](int ti, unsigned slot, unsigned ko) {
        const unsigned l = slot + ti * 4096 + w * 1024;
        if (ti < 4) dma16(l, vox, rx, sox + (64 * (ti >> 1) + 16 * (ti & 1)) * 1280 + ko);
        else dma16(l, vob, (ti & 1) ? rbim : rbre, sob + 32 * ((ti - 4) >> 1) * 1280 + ko);
    };
    if (r < 20) {
        const void* const pp[3] = {p.lre, p.lim, p.ldt};
#pragma unroll
        for (int k = 0; k < 3; ++k)
            asm volatile("s_mov_b32 m0, %0\n\ts_nop 0\n\tbuffer_load_dword %1, %2, %3 offen lds" ::"s"(A_PAR + w * 1024 + k * 256),
                         "v"(lane * 4), "s"(mk(pp[k], VBYTES)), "s"((64 * g + 32 * (w >> 1)) * 2) : "memory");
        asm volatile("s_mov_b32 m0, %0\n\ts_nop 0\n\tbuffer_load_dword %1, %2, 0 offen sc1 lds" ::"s"(A_PAR + w * 1024 + 768),
                     "v"(lane < 2 ? lane * 4 : 0x40000000), "s"(ws) : "memory");
        TOUCH(lane < 32 && w < 3 ? lane * 1280 : 0x40000000, mk((r & 2) ? p.bim : p.bre, MBYTES), (64 * g + 32 * (r & 1)) * 1280 + w * 128);
        TOUCH(lane < 26 && w < 3 ? lane * 1280 : 0x40000000, rx, (128 * c + ((r >> 2) * 51 >> 1)) * 1280 + w * 128);
#pragma unroll
        for (int ti = 0; ti < 8; ++ti) dma_piece(ti, 0, 0);
    } else if (r < 30) {
        const int jj = r - 20, grp = xl * 10 + jj, tb = 2 * (xl & 3) + (xl >> 2);
        const unsigned bw = w * BW_STRIDE, po = (grp * 8 + 2 * w) * 256;
        const void* const pp[6] = {p.sare, p.saim, p.sbre, p.sbim, p.scre, p.scim};
#pragma unroll
        for (int k = 0; k < 6; ++k)
            asm volatile("s_mov_b32 m0, %0\n\ts_nop 0\n\tbuffer_load_dwordx4 %1, %2, %3 offen nt lds" ::"s"(bw + BW_PAR + k * 1024),
                         "v"(lane < 32 ? lane * 16 : 0x40000000), "s"(mk(pp[k], SBYTES)), "s"(po) : "memory");
        asm volatile("s_mov_b32 m0, %0\n\ts_nop 0\n\tbuffer_load_dword %1, %2, 0 offen sc1 lds" ::"s"(bw + BW_PAR + 6144),
                     "v"(lane < 2 ? lane * 4 : 0x40000000), "s"(ws) : "memory");
        const int rb = lane >> 3;
#pragma unroll
        for (int i = 0; i < 8; ++i) {
            const unsigned f = (((rb >> 1) & 1) | ((i & 1) << 1)) << 1;
            dma16(bw + i * 1024, rb * 1280 + (((lane & 7) ^ f) * 16), rx, (256 * tb + 64 * w + 8 * i) * 1280 + jj * 128);
        }
    }
    int role = r;
    asm volatile("" : "+s"(role));

    if (__builtin_expect(role < 20, 1)) {
        const int th = w & 1, cg = w >> 1, ch = lane & 31, hh = lane >> 5;
        const int pch = 64 * g + 32 * cg + ch;
        STAMP(11);
        const int c2 = 2 * (2 * (xl & 3) + (xl >> 2)) + (r & 1), ct = r >> 1;
        CStage cs;
        cs.r0 = mk((w & 1) ? p.cim : p.cre, MBYTES); cs.r1 = mk((w & 1) ? p.cre : p.cim, MBYTES); cs.w = w;
#pragma unroll
        for (int e = 0; e < 2; ++e) {
            cs.vo[e] = (64 * ct + (lane >> 3)) * 1280 + (((lane & 7) ^ ((4 * e + (lane >> 4)) & 7)) * 16);
            cs.ra[e] = (lane & 15) * 128 + (((4 * e + (lane >> 4)) ^ ((lane >> 1) & 7)) * 16);
        }
        v4u cf[40];
        const int rot = 2 * (w + ct), own = rot & 7, c4 = lane & 3;
        const unsigned eb = 135168 + w * 5120;
        auto e_dma = [&](int i) {
            if (i < 4) dma16(eb + i * 1024, (lane >> 3) * 1280 + (((lane & 7) ^ (((lane >> 4) & 3) * 2)) * 16), rx, (128 * c2 + 16 * own + 8 * i) * 1280 + ct * 128);
            else asm volatile("s_mov_b32 m0, %0\n\ts_nop 0\n\tbuffer_load_dword %1, %2, %3 offen lds" ::"s"(eb + 4096 + (i - 4) * 256),
                              "v"(lane * 4), "s"(mk(i == 4 ? p.dska : p.dskb, VBYTES)), "s"(ct * 128) : "memory");
        };
        WAITV(8 + 2 * APF);
        const unsigned vlr = lds_ld<u16>(A_PAR + w * 1024 + ch * 2), vli = lds_ld<u16>(A_PAR + w * 1024 + 256 + ch * 2),
                       vld = lds_ld<u16>(A_PAR + w * 1024 + 512 + ch * 2);
        {
            const v2u ep = lds_ld<v2u>(A_PAR + w * 1024 + 768);
            tA = rfl(ep.x) + 1; tB = rfl(ep.y) + 1;
        }
        cpx lam, S2;
        {
            const float dt = rbf(__expf(bflo(vld))), Lr = bflo(vlr), Li = bflo(vli);
            const float e = __expf(dt * Lr), fr = __builtin_amdgcn_fractf((dt * Li) * 0.15915494309189535f);
            lam = {e * __builtin_amdgcn_cosf(fr), e * __builtin_amdgcn_sinf(fr)};
            const float nr = lam.r - 1.f, ni = lam.i, den = 2.f * __builtin_amdgcn_rcpf(Lr * Lr + Li * Li);
            S2 = {(nr * Lr + ni * Li) * den, (ni * Lr - nr * Li) * den};
        }
        PIN2(lam.r, lam.i); PIN2(S2.r, S2.i);
        const int seg = 2 * th + hh;
        float qr[32], qi[32];
        cpx pw[10];
        qr[0] = 1.f; qi[0] = 0.f; qr[1] = lam.r; qi[1] = lam.i;
        cpx sel0, sel1, sel2, sel3;
        auto tbl = [&](auto H) {
            constexpr int h = H.v, k = h >> 1;
            cpx a, bq, d;
            if constexpr (k < 30) {
                constexpr int n = k + 2, hb = n < 4 ? 2 : n < 8 ? 4 : n < 16 ? 8 : 16;
                constexpr int ia = n == hb ? hb / 2 : n - hb, ib = n == hb ? hb / 2 : hb;
                a = {qr[ia], qi[ia]}; bq = {qr[ib], qi[ib]};
                if constexpr (h & 1) d = {qr[n], qi[n]};
            } else {
                if constexpr (h == 70) {
                    sel0 = hh ? pw[0] : cpx{1.f, 0.f}; sel1 = th ? pw[1] : cpx{1.f, 0.f};
                    sel2 = hh ? pw[2] : cpx{1.f, 0.f}; sel3 = th ? pw[3] : cpx{1.f, 0.f};
                }
                if constexpr (k == 30) a = bq = {qr[16], qi[16]};
                else if constexpr (k < 35) a = bq = pw[k - 31];
                else if constexpr (k == 35) { a = sel0; bq = sel1; }
                else if constexpr (k == 36) { a = sel2; bq = sel3; }
                else { a = S2; bq = lam; }
                if constexpr (h & 1) d = pw[k - 30];
            }
            if constexpr (h & 1) CMUL_B(d.r, d.i, a.i, bq.r, bq.i); else CMUL_A(d.r, d.i, a.r, bq.r, bq.i);
            if constexpr (k < 30) { qr[k + 2] = d.r; qi[k + 2] = d.i; } else pw[k - 30] = d;
        };

        f32x16 aR[2], aI[2];
        unsigned adA[4], adB[4];
        {
            const unsigned lb = ch * 128 + ((hh ^ ((ch >> 1) & 7)) * 16);
#pragma unroll
            for (int k = 0; k < 4; ++k) { adA[k] = (lb ^ (k * 32)) + th * 8192; adB[k] = (lb ^ (k * 32)) + 16384 + cg * 8192; }
        }
        STAMP(1);
#pragma unroll
        for (int ti = 0; ti < 8; ++ti) dma_piece(ti, 32768, 128);
        asm volatile("s_waitcnt vmcnt(8)\n\ts_barrier" ::: "memory");
        STAMP(10);
        v4u fa0 = lds_ld<v4u>(adA[0]), fa1 = lds_ld<v4u>(adA[0] + 4096), fr = lds_ld<v4u>(adB[0]), fi = lds_ld<v4u>(adB[0] + 4096);
        const unsigned cb0 = 98304 + w * 8192, cb1 = 65536 + w * 8192;
        sfor<0, 40>([&](auto SK) {
            constexpr int s = SK.v >> 2, k = SK.v & 3;
            v4u na0 = fa0, na1 = fa1, nr = fr, ni = fi;
            if constexpr (k == 3 && s <= 8) asm volatile("s_waitcnt vmcnt(6) lgkmcnt(0)\n\ts_barrier" ::: "memory");
            constexpr int kn = (k + 1) & 3;
            constexpr unsigned so = ((k == 3 ? s + 1 : s) % 3) * 32768;
            constexpr bool rd = SK.v < 39;
            auto req = [&](auto N) {
                if constexpr (s <= 7) dma_piece(N.v, ((s + 2) % 3) * 32768, (s + 2) * 128);
                else if constexpr (s == 8) c_dma(cs, 0, N.v, cb0);
                else if constexpr (N.v < 6) e_dma(N.v);
            };
            aR[0] = mfma32<SK.v == 0>(fa0, fr, aR[0]); SB();
            if constexpr (rd) { na0 = lds_ld<v4u>(adA[kn] + so); nr = lds_ld<v4u>(adB[kn] + so); }
            if constexpr (2 * SK.v < 76) tbl(IC<2 * SK.v>{});
            SB();
            aI[0] = mfma32<SK.v == 0>(fa0, fi, aI[0]); SB();
            if constexpr (rd) { ni = lds_ld<v4u>(adB[kn] + so + 4096); na1 = lds_ld<v4u>(adA[kn] + so + 4096); }
            if constexpr (2 * SK.v + 1 < 76) tbl(IC<2 * SK.v + 1>{});
            SB();
            aR[1] = mfma32<SK.v == 0>(fa1, fr, aR[1]);
            req(IC<2 * k>{});
            aI[1] = mfma32<SK.v == 0>(fa1, fi, aI[1]);
            req(IC<2 * k + 1>{});
            SB();
            fa0 = na0; fa1 = na1; fr = nr; fi = ni;
        });
        f32x2 Qr[16], Qi[16];
#pragma unroll
        for (int i = 0; i < 16; ++i) { Qr[i] = f32x2{qr[2 * i], qr[2 * i + 1]}; Qi[i] = f32x2{qi[2 * i], qi[2 * i + 1]}; PIN2(Qr[i], Qi[i]); }
        const unsigned xch = 159744 + (cg * 2) * 256 + ch * 8;
        unsigned aggo = (hh == 0 && th == 1 && c < 15) ? WS_AGG + (c * 640 + pch) * 16 : WS_END;
        unsigned xchw = xch + th * 256;
        PIN2(aggo, xchw);
        asm volatile("s_nop 11" : "+v"(aR[0]), "+v"(aI[0]), "+v"(aR[1]), "+v"(aI[1]));
        STAMP(2);
        const cpx l32 = pw[0], l64 = pw[1], l512 = pw[4], lseg = pw[5], lgat = pw[6], Sl = pw[7];
        cpx agg;
        {
            f32x2 sr0 = {0.f, 0.f}, si0 = {0.f, 0.f}, sr1 = {0.f, 0.f}, si1 = {0.f, 0.f};
#pragma unroll
            for (int i = 0; i < 8; ++i) {
                const f32x2 ur = {aR[0][2 * i], aR[0][2 * i + 1]}, ui = {aI[0][2 * i], aI[0][2 * i + 1]};
                const f32x2 q0 = Qr[15 - i].yx, q1 = Qi[15 - i].yx;
                sr0 += q0 * ur; si0 += q0 * ui; sr0 -= q1 * ui; si0 += q1 * ur;
                const f32x2 vr = {aR[1][2 * i], aR[1][2 * i + 1]}, vi = {aI[1][2 * i], aI[1][2 * i + 1]};
                const f32x2 p0 = Qr[7 - i].yx, p1 = Qi[7 - i].yx;
                sr1 += p0 * vr; si1 += p0 * vi; sr1 -= p1 * vi; si1 += p1 * vr;
            }
            sr0 += sr1; si0 += si1;
            agg = {sr0.x + sr0.y, si0.x + si0.y};
        }
        cpx a0, a1;
        halves(agg.r, a0.r, a1.r); halves(agg.i, a0.i, a1.i);
        const cpx a64 = cfma(l32, a0, a1);
        lds_st<f32x2>(xchw, f32x2{a64.r, a64.i});
        BARRIER();
        const f32x2 o64v = lds_ld<f32x2>(xch);
        const cpx o64 = {o64v.x, o64v.y};
        {
            const cpx a128 = cfma(l64, o64, a64);
            __builtin_amdgcn_raw_buffer_store_b128(v4u{__float_as_uint(a128.r), tA, __float_as_uint(a128.i), tB}, ws, aggo, 0, AUX_SC1);
        }
        SB();
        STAMP(3);
        cpx pre;
        {
            const cpx z = {th ? o64.r : 0.f, th ? o64.i : 0.f}, t = cfma(l32, z, a0);
            pre = {hh ? t.r : z.r, hh ? t.i : z.i};
        }
        PIN2(pre.r, pre.i);
        {
            const float nli = -lam.i;
#pragma unroll
            for (int t = 1; t < 32; ++t) {
                float xr = aR[t >> 4][t & 15], xi = aI[t >> 4][t & 15];
                asm volatile("v_fmac_f32 %0, %4, %2\n\tv_fmac_f32 %1, %4, %3\n\tv_fmac_f32 %0, %6, %3\n\tv_fmac_f32 %1, %5, %2"
                             : "+v"(xr), "+v"(xi) : "v"(aR[(t - 1) >> 4][(t - 1) & 15]), "v"(aI[(t - 1) >> 4][(t - 1) & 15]), "v"(lam.r), "v"(lam.i), "v"(nli));
                aR[t >> 4][t & 15] = xr; aI[t >> 4][t & 15] = xi;
                if (t % 4 == 1) c_dma(cs, 1, t / 4, cb1);
            }
            WAITV(9);
#pragma unroll
            for (int f = 0; f < 8; ++f) c_read(cs, cf, 0, f, cb0);
            SB();
#pragma unroll
            for (int i = 0; i < 16; ++i) {
                const int t = 2 * i;
                const f32x2 gr = {aR[t >> 4][t & 15], aR[t >> 4][(t & 15) + 1]}, gi = {aI[t >> 4][t & 15], aI[t >> 4][(t & 15) + 1]};
                f32x2 hr = gr * S2.r - gi * S2.i, hi = -gi * S2.r - gr * S2.i;
                PIN2(hr, hi);
                aR[t >> 4][t & 15] = hr.x; aR[t >> 4][(t & 15) + 1] = hr.y; aI[t >> 4][t & 15] = hi.x; aI[t >> 4][(t & 15) + 1] = hi.y;
                if (i == 4) asm volatile("s_waitcnt lgkmcnt(0)" ::: "memory");
                if (i >= 5 && i < 13) c_dma(cs, 2, i - 5, cb0);
            }
        }
        const unsigned hs = w * 8192;
        unsigned wbx = hs + ch * 128 + (((8 * hh) ^ (((ch >> 1) & 1) << 2 | ((ch >> 3) & 1) << 3 | (ch & 1) | ((ch >> 2) & 1) << 1)) * 8);
        unsigned rad[2];
#pragma unroll
        for (int hf = 0; hf < 2; ++hf) {
            const int row = 8 * (lane >> 4) + 4 * hf + ((lane >> 2) & 3);
            const unsigned gs = ((row >> 1) & 1) << 2 | ((row >> 3) & 1) << 3 | (row & 1) | ((row >> 2) & 1) << 1;
            rad[hf] = hs + row * 128 + (((lane & 3) ^ gs) * 8);
        }
        unsigned wa[8], ra8[8];
#pragma unroll
        for (int q = 0; q < 8; ++q) { wa[q] = wbx ^ (q * 8); ra8[q] = rad[q & 1] ^ ((q >> 1) * 32); PIN2(wa[q], ra8[q]); }
        unsigned gof[4];
#pragma unroll
        for (int i = 0; i < 4; ++i) {
            const int j = c - 1 - (4 * i + seg);
            gof[i] = j < 0 ? WS_END : WS_AGG + (j * 640 + pch) * 16;
            PIN1(gof[i]);
        }
        int tok = 128 * c2 + 16 * own + (lane >> 2);
        PIN1(tok);
        const v2u gfl = __builtin_amdgcn_raw_buffer_load_b64(ws, WS_FLAG + (FL_G + c2) * 256, 0, AUX_SC1);
        MEMFENCE();
        cpx carry = {0.f, 0.f};
        if (c > 0) {
            v4u ga[4];
            for (int spin = 0; spin < SPIN_CAP; ++spin) {
                MEMFENCE();
                bool ok = true;
#pragma unroll
                for (int i = 0; i < 4; ++i) ga[i] = __builtin_amdgcn_raw_buffer_load_b128(ws, gof[i], 0, AUX_SC1);
#pragma unroll
                for (int i = 0; i < 4; ++i) ok = ok & ((gof[i] == WS_END) | ((ga[i].y == tA) & (ga[i].w == tB)));
                if (__all(ok)) break;
            }
            cpx part = {__uint_as_float(ga[3].x), __uint_as_float(ga[3].z)};
#pragma unroll
            for (int i = 2; i >= 0; --i) part = cfma(l512, part, cpx{__uint_as_float(ga[i].x), __uint_as_float(ga[i].z)});
            part = cmul(lgat, part);
            cpx p0, p1;
            halves(part.r, p0.r, p1.r); halves(part.i, p0.i, p1.i);
            const cpx ps = {p0.r + p1.r, p0.i + p1.i};
            lds_st<f32x2>(xchw + 1024, f32x2{ps.r, ps.i});
            BARRIER();
            const f32x2 o = lds_ld<f32x2>((xchw ^ 256) + 1024);
            carry = {ps.r + o.x, ps.i + o.y};
        }
        STAMP(4);
        if (!__all((gfl.x == tA) & (gfl.y == tB))) poll_flags(ws, WS_FLAG + (FL_G + c2) * 256, tA, tB);
        __builtin_amdgcn_fence(__ATOMIC_ACQUIRE, "wavefront");
        MEMFENCE();
        float gate[2], omg[2];
#pragma unroll
        for (int e = 0; e < 2; ++e) gate[e] = __uint_as_float(__builtin_amdgcn_raw_buffer_load_b32(ws, WS_G + tok * 4, e * 64, AUX_SC1));
        MEMFENCE();
        const cpx F = cmul(Sl, cfma(lseg, carry, pre));
        const unsigned cb2 = 32768 + w * 8192;
        const unsigned hso = WS_H + ((2 * c + th) * 160 + 4 * g + cg) * 1024;
#pragma unroll
        for (int hv = 0; hv < 2; ++hv) {
#pragma unroll
            for (int q = 4 * hv; q < 4 * hv + 4; ++q) {
                f32x2 hr[2], hi[2];
#pragma unroll
                for (int e = 0; e < 2; ++e) {
                    const int t = 4 * q + 2 * e;
                    const f32x2 ur = {aR[t >> 4][t & 15], aR[t >> 4][(t & 15) + 1]}, ui = {aI[t >> 4][t & 15], aI[t >> 4][(t & 15) + 1]};
                    const f32x2 q0 = Qr[t >> 1], q1 = Qi[t >> 1];
                    hr[e] = ur + q0 * F.r - q1 * F.i;
                    hi[e] = ui - q0 * F.i - q1 * F.r;
                }
                lds_st<v2u>(wa[q], v2u{pk2v(hr[0]), pk2v(hr[1])});
                lds_st<v2u>(wa[q] + 4096, v2u{pk2v(hi[0]), pk2v(hi[1])});
                SB();
                if (hv == 0) { c_dma(cs, 3, 2 * q, cb2); c_dma(cs, 3, 2 * q + 1, cb2); }
                SB();
            }
            v4u hq[4];
#pragma unroll
            for (int i = 0; i < 4; ++i) {
                const int mt = hv + 2 * (i & 1);
                const v2u x0 = lds_tr(ra8[2 * mt] + (i >> 1) * 4096), x1 = lds_tr(ra8[2 * mt + 1] + (i >> 1) * 4096);
                hq[i] = v4u{x0.x, x0.y, x1.x, x1.y};
            }
            SB();
#pragma unroll
            for (int i = 0; i < 4; ++i)
                __builtin_amdgcn_raw_buffer_store_b128(hq[i], ws, lane * 16, hso + (hv + 2 * (i & 1)) * 40960 + (i >> 1) * 2048, AUX_SC1);
            asm volatile("s_nop 1");
            SB();
        }
        STAMP(13);
#pragma unroll
        for (int f = 0; f < 8; ++f) { c_read(cs, cf, 1, f, cb1); c_read(cs, cf, 2, f, cb0); }
        float o[2][16];
        {
            const unsigned xa = eb + (lane >> 2) * 128 + (c4 & 1) * 8, sw = ((lane >> 3) & 3) * 2;
#pragma unroll
            for (int e = 0; e < 2; ++e) omg[e] = rbf(1.f - gate[e]);
#pragma unroll
            for (int nt = 0; nt < 4; ++nt) {
                const v2u av = lds_ld<v2u>(eb + 4096 + nt * 32 + c4 * 8), bv = lds_ld<v2u>(eb + 4352 + nt * 32 + c4 * 8);
#pragma unroll
                for (int e = 0; e < 2; ++e) {
                    const v2u xv = lds_ld<v2u>(xa + e * 2048 + (((2 * nt + (c4 >> 1)) ^ sw) * 16));
#pragma unroll
                    for (int k = 0; k < 2; ++k) {
                        const unsigned x1 = xv[k], a1 = av[k], b1 = bv[k];
                        f32x2 t = f32x2{bflo(x1), bfhi(x1)} * (f32x2{bflo(a1), bfhi(a1)} * gate[e] + f32x2{bflo(b1), bfhi(b1)} * omg[e]);
                        PIN1(t);
                        o[e][4 * nt + 2 * k] = t.x; o[e][4 * nt + 2 * k + 1] = t.y;
                    }
                }
            }
        }
        const unsigned pwo = (lane & 15) * 64 + (((lane >> 4) ^ ((lane >> 1) & 3)) * 16);
        const unsigned pro = (lane >> 2) * 64 + (((lane & 3) ^ ((lane >> 3) & 3)) * 16);
        const unsigned priv = 98304 + w * 8192;
        unsigned so[8], pc[8], pr[3];
#pragma unroll
        for (int q = 0; q < 8; ++q) {
            const unsigned T = (rot + 2 + q) & 7;
            so[q] = WS_H + ((8 * c2 + T) * 40 + 10 * w) * 1024;
            asm volatile("" : "+s"(so[q]));
            pc[q] = T * 4096 + lane * 16; PIN1(pc[q]);
        }
        pr[0] = priv + pwo; pr[1] = priv + pro;
        unsigned yfo = WS_FLAG + (FL_Y + ct * 8 + (lane & 7)) * 256;
        PIN1(yfo);
        unsigned ywa = priv + 4096 + (lane >> 3) * 128 + (((lane & 7) + 2 * ((lane >> 4) & 3)) & 7) * 16;
        pr[2] = priv + 4096 + c4 * 256 + (((lane >> 2) * 4 + 32 * c4) & 127);
        PIN2(pr[0], pr[1]); PIN2(pr[2], ywa);
        asm volatile("s_waitcnt vmcnt(0) lgkmcnt(0)\n\ts_barrier" ::: "memory");
        set_flag(ws, FL_H + c * 10 + g, tA, tB);
        STAMP(5);
        MEMFENCE();
#pragma unroll
        for (int i = 0; i < 8; ++i) c_dma(cs, 4, i, cb1);
#pragma unroll
        for (int f = 0; f < 8; ++f) c_read(cs, cf, 3, f, cb2);
        poll_flags(ws, WS_FLAG + (FL_H + c2 * 10 + ((10 * w) >> 2) + (lane < 3 ? lane : 0)) * 256, tA, tB);
        STAMP(6);
        constexpr int WIN = HWIN;
        v4u hv[WIN];
        unsigned hvo[3] = {(unsigned)lane * 16, (unsigned)lane * 16 + 4096, (unsigned)lane * 16 + 8192};
        PIN2(hvo[1], hvo[2]);
        auto h_load = [&](int i) { return __builtin_amdgcn_raw_buffer_load_b128(ws, hvo[(i % 10) >> 2] + ((i % 10) & 3) * 1024, so[i / 10], AUX_SC1); };
#pragma unroll
        for (int i = 0; i < WIN; ++i) { hv[i] = h_load(i); SB(); }
#pragma unroll
        for (int f = 0; f < 8; ++f) c_read(cs, cf, 4, f, cb1);
        SB();
        f32x4 acc[3][4];
        v2u yfl;
        v4u yq[4];
        bool yearly = false;
        auto y_load = [&]() {
            __builtin_amdgcn_fence(__ATOMIC_ACQUIRE, "wavefront");
            MEMFENCE();
#pragma unroll
            for (int i = 0; i < 4; ++i)
                yq[i] = __builtin_amdgcn_raw_buffer_load_b128(ws, (lane >> 3) * 8192 + (lane & 7) * 16,
                                                              WS_Y + (32 * ct + 8 * i) * 8192 + (128 * c2 + 16 * own) * 4, AUX_SC1);
        };
        auto add_part = [&](int e, int nt, const f32x4& v) {
#pragma unroll
            for (int k = 0; k < 4; ++k) asm volatile("v_fmac_f32 %0, %1, %2" : "+v"(o[e][4 * nt + k]) : "v"(gate[e]), "v"(v[k]));
        };
        sfor<0, 80>([&](auto I) {
            constexpr int i = I.v, q = i / 10, j = i % 10;
            const v4u hf = hv[i % WIN];
            sfor<0, 4>([&](auto NT) {
                constexpr int nt = NT.v;
                if constexpr (j == 0 && q < 2) acc[q % 3][nt] = mfma16z(cf[nt], hf); else acc[q % 3][nt] = mfma16(cf[4 * j + nt], hf, acc[q % 3][nt]);
                SB();
                if constexpr (nt == 0 && i >= 1 && i - 1 + WIN < 80) hv[(i - 1) % WIN] = h_load(i - 1 + WIN);
                if constexpr (nt == 1 && i == 60) yfl = __builtin_amdgcn_raw_buffer_load_b64(ws, yfo, 0, AUX_SC1);
                if constexpr (nt == 2 && q > 0 && j >= 1 && j < 5) lds_st<f32x4>((q < 7 ? pc[q - 1] : pr[0]) + (j - 1) * 1024, acc[(q - 1) % 3][j - 1]);
                if constexpr (nt == 2 && q >= 1 && q < 7 && j >= 5 && j < 9) acc[(q + 1) % 3][j - 5] = lds_ld<f32x4>(pc[q + 1] + (j - 5) * 1024);
                SB();
            });
            if constexpr (j == 4 && q < 7) BARRIER();
            if constexpr (i == 72) { yearly = __all((yfl.x == tA) & (yfl.y == tB)); if (yearly) y_load(); }
            SB();
        });
        STAMP(7);
        if (!yearly) { poll_flags(ws, yfo, tA, tB); y_load(); }
        SB();
        {
            f32x4 f0[4], f1[4];
#pragma unroll
            for (int nt = 0; nt < 4; ++nt) f0[nt] = lds_ld<f32x4>(pr[1] + nt * 1024);
#pragma unroll
            for (int nt = 0; nt < 4; ++nt) lds_st<f32x4>(pr[0] + nt * 1024, acc[7 % 3][nt]);
#pragma unroll
            for (int nt = 0; nt < 4; ++nt) f1[nt] = lds_ld<f32x4>(pr[1] + nt * 1024);
            SB();
#pragma unroll
            for (int nt = 0; nt < 4; ++nt) add_part(0, nt, f0[nt]);
#pragma unroll
            for (int nt = 0; nt < 4; ++nt) add_part(1, nt, f1[nt]);
        }
        SB();
#pragma unroll
        for (int i = 0; i < 4; ++i) lds_st<v4u>(ywa + i * 1024, yq[i]);
        STAMP(8);
        const rsrc_t ro = mk(p.out, SEQ * DM * 4);
#pragma unroll
        for (int e = 0; e < 2; ++e) {
            f32x4 oo[4];
#pragma unroll
            for (int i = 0; i < 8; ++i) {
                const unsigned yv = lds_ld<unsigned>((pr[2] ^ (e * 64)) + (i >> 1) * 1024 + (i & 1) * 128);
                const f32x2 t = f32x2{o[e][2 * i], o[e][2 * i + 1]} + f32x2{bflo(yv), bfhi(yv)} * omg[e];
                oo[i >> 1][2 * (i & 1)] = t.x; oo[i >> 1][2 * (i & 1) + 1] = t.y;
            }
            PIN2(oo[0], oo[1]); PIN2(oo[2], oo[3]);
            SB();
#pragma unroll
            for (int nt = 0; nt < 4; ++nt)
                __builtin_amdgcn_raw_buffer_store_b128(__builtin_bit_cast(v4u, oo[nt]), ro, tok * 2560 + c4 * 16, ct * 256 + nt * 64 + e * 16 * 2560, 2);
            asm volatile("s_nop 1");
            SB();
        }
        CODE_MARK(a);
    } else if (__builtin_expect(role < 30, 1)) {
        const int jj = r - 20, grp = xl * 10 + jj, H = xl >> 2, tb = 2 * (xl & 3) + H;
        const unsigned bw = w * BW_STRIDE;
        STAMP(10);
        const unsigned ldt2 = *(const __attribute__((address_space(4))) unsigned*)((uintptr_t)p.sldt + (grp * 8 + 2 * w) * 2);
        const rsrc_t rpf = mk(jj < 5 ? p.bre : p.bim, MBYTES);
        const unsigned pfo = (320 * H + 64 * (jj < 5 ? jj : jj - 5)) * 1280;
        const rsrc_t rc0 = mk((w & 1) ? p.cim : p.cre, MBYTES), rc1 = mk((w & 1) ? p.cre : p.cim, MBYTES);
        KgAddr ka;
        {
            const int j = lane & 31, hh = lane >> 5;
            const unsigned tab = bw + BW_TAB;
            ka.ra = bw + BW_STG + hh * STG_CH + j * 16;
            ka.t0 = tab + (8 * j + 4 * hh) * 2;
            ka.t1 = tab + (hh ? 384 + 2 * j : 256 + 4 * j) * 2; ka.t2 = tab + (hh ? 448 + 2 * j : 258 + 4 * j) * 2;
            ka.t3 = tab + (hh ? 640 + j : 512 + j) * 2;
            ka.tb = tab - 2 * 0x4280;
            ka.wa = bw + (lane & 3) * KEXP_RHO + (lane >> 2) * 16;
            const int lb = 2044 + (lane & 3) - 32 * (lane >> 2);
            f32x2 lf = {(float)lb, (float)(lb - 4)};
            PIN1(lf);
#pragma unroll
            for (int n = 0; n < 16; ++n) {
                const int o = 512 * (n >> 2) + 8 * (n & 3);
                unsigned pk = pk2v(lf - (float)o);
                if (n >= 12 && lb - o < 256) pk = (unsigned)(lb - o + 0x4280) | (unsigned)(lb - o - 4 + 0x4280) << 16;
                ka.lag[n] = pk;
                PIN1(ka.lag[n]);
            }
        }
        PIN2(ka.ra, ka.t0); PIN2(ka.t1, ka.t2); PIN2(ka.t3, ka.tb); PIN1(ka.wa);
        unsigned sa = bw + BW_STG + (lane >> 1) * STG_CH + (lane & 1) * 8, xta[4];
#pragma unroll
        for (int cs = 0; cs < 4; ++cs) {
            const int row = 8 * (lane >> 4) + ((lane >> 2) & 3);
            const unsigned f = (((row >> 1) & 1) | (((row >> 3) & 1) << 1)) << 1;
            xta[cs] = bw + row * 128 + (((2 * cs + ((lane & 3) >> 1)) ^ f) * 16) + (lane & 1) * 8;
            PIN1(xta[cs]);
        }
        unsigned xvo = ((lane >> 3) & 1) * 32768 + (lane & 7) * 16 + (lane >> 4) * 128;
        unsigned ua = ((lane & 7) >> 1) * BW_STRIDE + BW_U + (lane & 1) * U_CH + 64 + (64 * w + (lane >> 3)) * 16;
        PIN2(sa, xvo); PIN1(ua);
        WAITV(8);
        KgIn in[2];
#pragma unroll
        for (int e = 0; e < 2; ++e) {
            const unsigned a = bw + BW_PAR + e * 256 + lane * 4;
            in[e].are = lds_ld<unsigned>(a); in[e].aim = lds_ld<unsigned>(a + 1024); in[e].bre = lds_ld<unsigned>(a + 2048);
            in[e].bim = lds_ld<unsigned>(a + 3072); in[e].cre = lds_ld<unsigned>(a + 4096); in[e].cim = lds_ld<unsigned>(a + 5120);
            in[e].ldt = e ? ldt2 >> 16 : ldt2 & 0xffffu;
        }
        {
            const v2u ep = lds_ld<v2u>(bw + BW_PAR + 6144);
            tA = rfl(ep.x) + 1; tB = rfl(ep.y) + 1;
        }
        STAMP(1);
        v4u xq[8];
        auto xt_store = [&](int i) {
            __builtin_amdgcn_raw_buffer_store_b128(xq[i], ws, xvo, WS_XT + (jj * 8 + 2 * (i & 3)) * 32768 + (tb * 8 + 2 * w + (i >> 2)) * 512, AUX_SC1);
            asm volatile("s_nop 1");
        };
        KgState ks = kg_lo(in[0], sa, [&](auto K) {
            if constexpr (K.v == 0) TOUCH(lane * 1280, rpf, pfo + (w + 3) * 128);
            if constexpr (K.v == 2) TOUCH(w < 3 ? lane * 1280 : 0x40000000, rpf, pfo + (w + 7) * 128);
            if constexpr (K.v == 3) {
                WAITV(2);
#pragma unroll
                for (int i = 0; i < 8; ++i) {
                    const v2u x0 = lds_tr(xta[i & 3] + (i >> 2) * 4096), x1 = lds_tr(xta[i & 3] + (i >> 2) * 4096 + 512);
                    xq[i] = v4u{x0.x, x0.y, x1.x, x1.y};
                }
            }
            if constexpr (K.v >= 4) xt_store(K.v - 4);
        });
        STAMP(2);
        kg_hi(ks, sa, [&](auto R) {
            if constexpr (R.v < 8) xt_store(4 + (R.v >> 1));
            if constexpr (R.v == 11) touch(lane < 40 ? (lane >> 2) * 128 + (lane & 3) * 1280 : 0x40000000, rc0, (grp * 8 + (w >> 1) * 4) * 1280);
        });
        asm volatile("s_waitcnt vmcnt(1)\n\ts_barrier" ::: "memory");
        set_flag(ws, FL_XT + tb * 10 + jj, tA, tB);
        STAMP(7);
        auto c_l2 = [&](int pb) {
            touch(lane * 1280, (pb & 1) ? rc1 : rc0, ((5 * w + pb) >> 1) * 128 + jj * 64 * 1280);
        };
        c_l2(0);
        kg_finish(ka, BW_K1, lane);
        STAMP(3);
        v4u uu[8];
        ks = kg_lo(in[1], sa, [&](auto K) {
            if constexpr (K.v == 0) {
                poll_flags(ws, WS_FLAG + (FL_XT + (lane & 7) * 10 + (grp >> 3)) * 256, tA, tB);
            }
            if constexpr (K.v < 4) {
#pragma unroll
                for (int i = 2 * K.v; i < 2 * K.v + 2; ++i)
                    uu[i] = __builtin_amdgcn_raw_buffer_load_b128(ws, lane * 16, WS_XT + grp * 32768 + w * 8192 + i * 1024, AUX_SC1);
            }
            if constexpr (K.v == 7) c_l2(1);
            if constexpr (K.v >= 4) { lds_st<v4u>(ua + (2 * K.v - 8) * 128, uu[2 * K.v - 8]); lds_st<v4u>(ua + (2 * K.v - 7) * 128, uu[2 * K.v - 7]); }
        });
        PIN2(ks.w.r, ks.w.i); PIN2(ks.e1.r, ks.e1.i); PIN2(ks.e2.r, ks.e2.i); PIN2(ks.e4.r, ks.e4.i); PIN2(ks.e256.r, ks.e256.i);
        if (lane < 8) lds_st<v4u>(bw + BW_U + (lane >> 2) * U_CH + (lane & 3) * 16, v4u{0, 0, 0, 0});
        BARRIER();
        STAMP(4);
        MEMFENCE();
        float y1[32], y2[32];
        {
            float hr[21][2], hi[21][2];
            hr[0][0] = ks.w.r.x; hr[0][1] = ks.w.r.y; hi[0][0] = ks.w.i.x; hi[0][1] = ks.w.i.y;
            f32x16 d;
            v4u ta[2], tb2[2];
            unsigned t[32];
            conv_channel<false>(bw + BW_K1, bw + BW_U, lane, y1, [&](auto M, auto FI) {
                constexpr int m = M.v, f = FI.v;
                if constexpr (f >= 0 && f < 52) {
                    constexpr int pr = f / 5, q = f % 5;
                    if constexpr (q < 4) {
                        constexpr int r = 2 * pr + 1 + (q >> 1), md = q & 1;
                        if constexpr (r < 21) {
                            const cp2& mu = hmul(ks, HMUL[r]);
                            const float br = md ? mu.r.y : mu.r.x, bi = md ? mu.i.y : mu.i.x;
                            const cpx o = cmulv(cpx{hr[HSRC[r]][md], hi[HSRC[r]][md]}, cpx{br, bi});
                            hr[r][md] = o.r; hi[r][md] = o.i;
                        }
                    } else {
                        lds_st<v2u>(sa + 2 * pr * 16, v2u{pk2(hr[2 * pr][0], hr[2 * pr][1]), pk2(hi[2 * pr][0], hi[2 * pr][1])});
                        lds_st<v2u>(sa + (2 * pr + 1) * 16, v2u{pk2(hr[2 * pr + 1][0], hr[2 * pr + 1][1]), pk2(hi[2 * pr + 1][0], hi[2 * pr + 1][1])});
                    }
                    if constexpr (f == 50) lds_st<v2u>(sa + 20 * 16, v2u{pk2(hr[20][0], hr[20][1]), pk2(hi[20][0], hi[20][1])});
                    if constexpr (f == 50 || f == 51) { ta[f & 1] = lds_ld<v4u>(ka.ra + (f - 50) * 2 * STG_CH); tb2[f & 1] = lds_ld<v4u>(ka.ra + (f - 50) * 2 * STG_CH + 21 * 16); }
                } else if constexpr (f >= 52 && f < 68) {
                    constexpr int k = f - 52;
                    d = mfma32<k == 0>(ta[k & 1], tb2[k & 1], d);
                    SB();
                    if constexpr (k < 14) { ta[k & 1] = lds_ld<v4u>(ka.ra + (k + 2) * 2 * STG_CH); tb2[k & 1] = lds_ld<v4u>(ka.ra + (k + 2) * 2 * STG_CH + 21 * 16); }
                } else if constexpr (f == 69) {
                    asm volatile("s_nop 7" : "+v"(d));
                } else if constexpr (f >= 70 && f < 74) {
                    kg_scatter(ka, d, f - 70);
                } else if constexpr (f >= 74 && f < 92) {
                    constexpr int n = 2 * (f - 74);
                    if constexpr (n < 32) { t[n] = lds_ld<u16>(tab_addr(ka, n)); t[n + 1] = lds_ld<u16>(tab_addr(ka, n + 1)); }
                    if constexpr (n >= 4) { t[n - 4] |= t[n - 3] << 16; PIN1(t[n - 4]); }
                    if constexpr (n >= 10 && (n - 10) % 8 == 0) {
                        constexpr int r = (n - 10) / 8;
                        lds_st<v4u>(ka.wa + BW_K2 + r * 256, v4u{t[8 * r], t[8 * r + 2], t[8 * r + 4], t[8 * r + 6]});
                    }
                } else if constexpr (f == 68) {
                    if (lane < 28) lds_st<v4u>(ka.wa + BW_K2 + 1024, v4u{0, 0, 0, 0});
                }
                if constexpr (m == 35) STAMP(13);
                if constexpr (m == 71) STAMP(14);
                if constexpr (m == 107) STAMP(15);
                if constexpr (m == 143) STAMP(8);
                if constexpr (m == 7) c_l2(2);
                if constexpr (m == 43) c_l2(3);
                if constexpr (m == 79) c_l2(4);
            });
        }
        STAMP(12);
        conv_channel<false>(bw + BW_K2, bw + BW_U + U_CH, lane, y2, [&](auto, auto) {});
        STAMP(5);
#pragma unroll
        for (int G = 0; G < 32; ++G) lds_st<unsigned>(bw + BW_Y + G * 256 + lane * 4, pk2(y1[G], y2[G]));
        {
            v4u yq[8];
#pragma unroll
            for (int i = 0; i < 8; ++i) yq[i] = lds_ld<v4u>(bw + BW_Y + i * 1024 + lane * 16);
            SB();
#pragma unroll
            for (int i = 0; i < 8; ++i)
                __builtin_amdgcn_raw_buffer_store_b128(yq[i], ws, lane * 16, WS_Y + (grp * 4 + w) * 8192 + i * 1024, AUX_SC1);
            asm volatile("s_nop 1");
            SB();
        }
        asm volatile("s_waitcnt vmcnt(0) lgkmcnt(0)\n\ts_barrier" ::: "memory");
        set_flag(ws, FL_Y + grp, tA, tB);
        STAMP(6);
        CODE_MARK(b);
    } else {
        const int gc = 2 * (2 * (xl & 3) + 1 - (xl >> 2)) + (r - 30);
        const v2u ep = __builtin_amdgcn_raw_buffer_load_b64(ws, WS_EPOCH, 0, AUX_SC1);
        const rsrc_t rgw = mk(p.gw, VBYTES);
        const unsigned vo = (lane >> 3) * 1280 + (lane & 7) * 16, so = (128 * gc + 32 * w) * 1280;
        float sum[4] = {0.f, 0.f, 0.f, 0.f};
        v4u gv[10], xv[10][4];
#pragma unroll
        for (int s = 0; s < 10; ++s) {
            gv[s] = __builtin_amdgcn_raw_buffer_load_b128(rgw, (lane & 7) * 16, ((s + 3) % 10) * 128, 0);
#pragma unroll
            for (int q = 0; q < 4; ++q) xv[s][q] = __builtin_amdgcn_raw_buffer_load_b128(rx, vo, so + q * 8 * 1280 + ((s + 3) % 10) * 128, PFNT ? 2 : 0);
        }
        SB();
#pragma unroll
        for (int s = 0; s < 10; ++s)
#pragma unroll
            for (int e = 0; e < 4; ++e)
#pragma unroll
                for (int q = 0; q < 4; ++q) {
                    const unsigned xa = xv[s][q][e], ga = gv[s][e];
                    sum[q] = __builtin_amdgcn_fdot2_f32_bf16(__builtin_bit_cast(bf16x2, xa), __builtin_bit_cast(bf16x2, ga), sum[q], false);
                }
        tA = rfl(ep.x) + 1; tB = rfl(ep.y) + 1;
#pragma unroll
        for (int q = 0; q < 4; ++q) {
            float v = sum[q];
            v += __shfl_xor(v, 1); v += __shfl_xor(v, 2); v += __shfl_xor(v, 4);
            const float z = rbf(v), gt = rbf(__builtin_amdgcn_rcpf(1.f + __expf(-z)));
            if ((lane & 7) == 0)
                __builtin_amdgcn_raw_buffer_store_b32(__float_as_uint(gt), ws, WS_G + (128 * gc + 32 * w + 8 * q + (lane >> 3)) * 4, 0, AUX_SC1);
        }
        asm volatile("s_waitcnt vmcnt(0) lgkmcnt(0)\n\ts_barrier" ::: "memory");
        set_flag(ws, FL_G + gc, tA, tB);
        STAMP(5);
        if (b == 255 && w == 0) {
            for (int k = 3; k >= 0; --k) {
                const int i = k * 64 + lane;
                const unsigned off = WS_FLAG + (i < 240 ? i : i + 80) * 256;
                for (int spin = 0; spin < SPIN_CAP; ++spin) {
                    MEMFENCE();
                    const v2u f = __builtin_amdgcn_raw_buffer_load_b64(ws, off, 0, AUX_SC1);
                    if (__all((f.x == tA) & (f.y == tB))) break;
                    __builtin_amdgcn_s_sleep(16);
                }
            }
            if (lane == 0) __builtin_amdgcn_raw_buffer_store_b64(v2u{tA, tB}, ws, WS_EPOCH, 0, AUX_SC1);
        }
        CODE_MARK(g);
    }
    asm volatile("s_waitcnt vmcnt(0)" ::: "memory");
    STAMP(9);
    if (tid < 16) ((GAS v4u*)((char*)p.ws + WS_STAMP))[blockIdx.x * 16 + tid] = lds_ld<v4u>(163328 + tid * 16);
    if (b == RWG && w == 0) {
        const v4u s0 = lds_ld<v4u>(163328 + RFROM * 16), s1 = lds_ld<v4u>(163328 + RTO * 16);
        unsigned d = RCYC ? (rfl(s1.z) - rfl(s0.z)) / 10 : (rfl(s1.x) - rfl(s0.x)) * 20;
        if (d > 100000u) d = 100000u;
        const u64 t0 = __builtin_amdgcn_s_memrealtime();
        while (__builtin_amdgcn_s_memrealtime() - t0 < d) __builtin_amdgcn_s_sleep(2);
    }
}

extern "C" void kernel_launch(void* const* d_in, const int* in_sizes, int n_in, void* d_out, int out_size, void* d_ws,
                              size_t ws_size, hipStream_t stream) {
    Params p;
    p.x = d_in[0]; p.lre = d_in[1]; p.lim = d_in[2]; p.bre = d_in[3]; p.bim = d_in[4]; p.cre = d_in[5]; p.cim = d_in[6];
    p.ldt = d_in[7]; p.dska = d_in[8];
    p.sare = d_in[10]; p.saim = d_in[11]; p.sbre = d_in[12]; p.sbim = d_in[13]; p.scre = d_in[14]; p.scim = d_in[15];
    p.sldt = d_in[16]; p.dskb = d_in[17]; p.gw = d_in[18];
    p.out = d_out; p.ws = d_ws;
    (void)hipFuncSetAttribute((const void*)k_fused, hipFuncAttributeMaxDynamicSharedMemorySize, LDS_BYTES);
    k_fused<<<256, 256, LDS_BYTES, stream>>>(p);
}
```

```cpp
#define HWIN 12
#include <hip/hip_runtime.h>
#include <stdint.h>

#define DIAG 0

#define DI __device__ __forceinline__
#define GAS __attribute__((address_space(1)))
#define LAS __attribute__((address_space(3)))
typedef unsigned short u16;
typedef unsigned long long u64;
typedef unsigned v4u __attribute__((ext_vector_type(4)));
typedef unsigned v2u __attribute__((ext_vector_type(2)));
typedef float f32x2 __attribute__((ext_vector_type(2)));
typedef float f32x4 __attribute__((ext_vector_type(4)));
typedef float f32x16 __attribute__((ext_vector_type(16)));
typedef short s16x4 __attribute__((ext_vector_type(4)));
typedef __bf16 bf16x2 __attribute__((ext_vector_type(2)));
typedef __amdgpu_buffer_rsrc_t rsrc_t;

constexpr int SEQ = 2048, DM = 640, NMODE = 128;
constexpr unsigned XBYTES = SEQ * DM * 2, MBYTES = DM * DM * 2, SBYTES = DM * NMODE * 2, VBYTES = DM * 2;

constexpr unsigned WS_EPOCH = 0;
constexpr unsigned WS_FLAG = 4096;
constexpr unsigned FL_H = 0, FL_XT = 160, FL_Y = 240, FL_G = 320, FL_N = 336;
constexpr unsigned WS_G = 96 * 1024;
constexpr unsigned WS_AGG = 128 * 1024;
constexpr unsigned WS_H = 512 * 1024;
constexpr unsigned WS_XT = WS_H + SEQ * 1280 * 2;
constexpr unsigned WS_Y = WS_XT + XBYTES;
constexpr unsigned WS_END = WS_Y + XBYTES;
constexpr unsigned WS_STAMP = 16u << 20;

constexpr int LDS_BYTES = 163840;
constexpr unsigned LDS_DUMMY = LDS_BYTES - 256;
constexpr int SPIN_CAP = 20000;
constexpr int AUX_SC1 = 16;

#define XFR 9
#define XPK 4
#define FLIP 0
#define RL 1
#define HPF 0
#define APF 2
#define BPF 1
#define PFNT 0
#define TOUCH touch
#define DMAX dma16
#define NT_STAGES 0
#define NC1 20
#define NC2 10

struct Params {
    const void *x, *lre, *lim, *bre, *bim, *cre, *cim, *ldt, *dska;
    const void *sare, *saim, *sbre, *sbim, *scre, *scim, *sldt, *dskb, *gw;
    void *out, *ws;
};

DI rsrc_t mk(const void* p, unsigned bytes) { return __builtin_amdgcn_make_buffer_rsrc((void*)p, (short)0, (int)bytes, 0x00020000); }
DI float bflo(unsigned v) { return __uint_as_float(v << 16); }
DI float bfhi(unsigned v) { return __uint_as_float(v & 0xffff0000u); }
DI unsigned pk2(float a, float b) { f32x2 v = {a, b}; return __builtin_bit_cast(unsigned, __builtin_convertvector(v, bf16x2)); }
DI unsigned pk2v(f32x2 v) { return __builtin_bit_cast(unsigned, __builtin_convertvector(v, bf16x2)); }
DI float rbf(float a) { return bflo(pk2(a, a)); }
DI unsigned rfl(unsigned v) { return __builtin_amdgcn_readfirstlane(v); }
template <int I> struct IC { static constexpr int v = I; };
template <int B, int E, class F> DI void sfor(F&& f) { if constexpr (B < E) { f(IC<B>{}); sfor<B + 1, E>(f); } }
#define SB() __builtin_amdgcn_sched_barrier(0)
#define MEMFENCE() asm volatile("" ::: "memory")

template <class T> struct Any { typedef T __attribute__((may_alias)) type; };
template <class T> DI T lds_ld(unsigned a) { return *(const LAS typename Any<T>::type*)(uintptr_t)a; }
template <class T> DI void lds_st(unsigned a, T v) { *(LAS typename Any<T>::type*)(uintptr_t)a = v; }
DI v2u lds_tr(unsigned a) { return __builtin_bit_cast(v2u, __builtin_amdgcn_ds_read_tr16_b64_v4i16((LAS s16x4*)(uintptr_t)a)); }

#define MFMA32(acc, a, b) asm volatile("v_mfma_f32_32x32x16_bf16 %0, %1, %2, %0" : "+v"(acc) : "v"(a), "v"(b))
#define MFMA32Z(acc, a, b) asm volatile("v_mfma_f32_32x32x16_bf16 %0, %1, %2, 0" : "=&v"(acc) : "v"(a), "v"(b))
template <bool Z> DI f32x16 mfma32(v4u a, v4u b, f32x16 c) {
    if constexpr (Z) asm volatile("v_mfma_f32_32x32x16_bf16 %0, %1, %2, 0" : "=&v"(c) : "v"(a), "v"(b));
    else asm volatile("v_mfma_f32_32x32x16_bf16 %0, %1, %2, %0" : "+v"(c) : "v"(a), "v"(b));
    return c;
}
DI f32x4 mfma16(v4u a, v4u b, f32x4 c) { asm volatile("v_mfma_f32_16x16x32_bf16 %0, %1, %2, %0" : "+v"(c) : "a"(a), "v"(b)); return c; }
DI f32x4 mfma16z(v4u a, v4u b) { f32x4 c; asm volatile("v_mfma_f32_16x16x32_bf16 %0, %1, %2, 0" : "=&v"(c) : "a"(a), "v"(b)); return c; }
#define MFMA16A(acc, a, b) asm volatile("v_mfma_f32_16x16x32_bf16 %0, %1, %2, %0" : "+v"(acc) : "a"(a), "v"(b))
#define MFMA16AZ(acc, a, b) asm volatile("v_mfma_f32_16x16x32_bf16 %0, %1, %2, 0" : "=&v"(acc) : "a"(a), "v"(b))

DI void dma16(unsigned ldsoff, unsigned voff, rsrc_t rs, unsigned soff) {
    asm volatile("s_mov_b32 m0, %0\n\ts_nop 0\n\tbuffer_load_dwordx4 %1, %2, %3 offen lds" ::"s"(ldsoff), "v"(voff), "s"(rs), "s"(soff) : "memory");
}
DI void dma16nt(unsigned ldsoff, unsigned voff, rsrc_t rs, unsigned soff) {
    asm volatile("s_mov_b32 m0, %0\n\ts_nop 0\n\tbuffer_load_dwordx4 %1, %2, %3 offen nt lds" ::"s"(ldsoff), "v"(voff), "s"(rs), "s"(soff) : "memory");
}
DI void touchnt(unsigned voff, rsrc_t rs, unsigned soff) {
    asm volatile("s_mov_b32 m0, %0\n\ts_nop 4\n\tbuffer_load_dword %1, %2, %3 offen nt lds" ::"s"(LDS_DUMMY), "v"(voff), "s"(rs), "s"(soff) : "memory");
}
DI void touch(unsigned voff, rsrc_t rs, unsigned soff) {
    asm volatile("s_mov_b32 m0, %0\n\ts_nop 4\n\tbuffer_load_dword %1, %2, %3 offen lds" ::"s"(LDS_DUMMY), "v"(voff), "s"(rs), "s"(soff) : "memory");
}
#define CODE_MARK(x) asm volatile(".Lkf_end_" #x ":")
#define LT(i) do {} while (0)
#define WAITV(n) asm volatile("s_waitcnt vmcnt(%0)" ::"n"(n) : "memory")
#define BARRIER() asm volatile("s_waitcnt lgkmcnt(0)\n\ts_barrier" ::: "memory")

#define STAMP(i) do {} while (0)

DI void poll_flags(rsrc_t ws, unsigned off, unsigned tA, unsigned tB) {
#pragma unroll 1
    for (int spin = 0; spin < SPIN_CAP; ++spin) {
        MEMFENCE();
        const v2u f = __builtin_amdgcn_raw_buffer_load_b64(ws, off, 0, AUX_SC1);
        if (__all((f.x == tA) & (f.y == tB))) break;
    }
    __builtin_amdgcn_fence(__ATOMIC_ACQUIRE, "wavefront");
    MEMFENCE();
}
DI void set_flag(rsrc_t ws, unsigned idx, unsigned tA, unsigned tB) {
    if (threadIdx.x == 0) __builtin_amdgcn_raw_buffer_store_b64(v2u{tA, tB}, ws, WS_FLAG + idx * 256, 0, AUX_SC1);
}

#define PIN1(a) asm volatile("" : "+v"(a))
#define PIN2(a, b) asm volatile("" : "+v"(a), "+v"(b))
#define CMUL_A(dr, di, ar, br, bi) asm volatile("v_mul_f32 %0, %2, %3\n\tv_mul_f32 %1, %2, %4" : "=&v"(dr), "=&v"(di) : "v"(ar), "v"(br), "v"(bi))
#define CMUL_B(dr, di, ai, br, bi) asm volatile("v_fma_f32 %0, -%2, %4, %0\n\tv_fmac_f32 %1, %2, %3" : "+v"(dr), "+v"(di) : "v"(ai), "v"(br), "v"(bi))
struct cpx { float r, i; };
DI cpx cmul(cpx a, cpx b) {
    cpx d;
    asm("v_mul_f32 %0, %2, %4\n\tv_mul_f32 %1, %2, %5\n\tv_fma_f32 %0, -%3, %5, %0\n\tv_fmac_f32 %1, %3, %4"
        : "=&v"(d.r), "=&v"(d.i) : "v"(a.r), "v"(a.i), "v"(b.r), "v"(b.i));
    return d;
}
DI cpx cmulv(cpx a, cpx b) {
    cpx d;
    asm volatile("v_mul_f32 %0, %2, %4\n\tv_mul_f32 %1, %2, %5\n\tv_fma_f32 %0, -%3, %5, %0\n\tv_fmac_f32 %1, %3, %4"
                 : "=&v"(d.r), "=&v"(d.i) : "v"(a.r), "v"(a.i), "v"(b.r), "v"(b.i));
    return d;
}
DI cpx cfma(cpx a, cpx b, cpx c) {
    cpx d;
    asm("v_fma_f32 %0, %2, %4, %6\n\tv_fma_f32 %1, %2, %5, %7\n\tv_fma_f32 %0, -%3, %5, %0\n\tv_fmac_f32 %1, %3, %4"
        : "=&v"(d.r), "=&v"(d.i) : "v"(a.r), "v"(a.i), "v"(b.r), "v"(b.i), "v"(c.r), "v"(c.i));
    return d;
}
struct cp2 { f32x2 r, i; };
DI cp2 cmul2(cp2 a, cp2 b) { return {a.r * b.r - a.i * b.i, a.r * b.i + a.i * b.r}; }
DI cp2 cmul2s(cp2 a, cpx b) { return {a.r * b.r - a.i * b.i, a.r * b.i + a.i * b.r}; }
DI void halves(float v, float& lo, float& hi) {
    auto s = __builtin_amdgcn_permlane32_swap(__float_as_uint(v), __float_as_uint(v), false, false);
    lo = __uint_as_float(s[0]); hi = __uint_as_float(s[1]);
}

struct CStage { rsrc_t r0, r1; unsigned vo[2], ra[2]; int w; };
DI void c_dma(const CStage& c, int pb, int i, unsigned buf) {
    const unsigned u = 5u * c.w + pb;
    dma16(buf + i * 1024, c.vo[i & 1], (pb & 1) ? c.r1 : c.r0, (u >> 1) * 128u + i * 8u * 1280u);
}
DI void c_read(const CStage& c, v4u (&cf)[40], int pb, int f, unsigned buf) {
    cf[8 * pb + f] = lds_ld<v4u>(buf + c.ra[f >> 2] + (f & 3) * 2048);
}

struct KgIn { unsigned are, aim, bre, bim, cre, cim, ldt; };
DI cp2 cexp2(f32x2 zr2, f32x2 zi2, float l) {
    const f32x2 a = zr2 * l, b = zi2 * l;
    cp2 o;
    const float m0 = __builtin_amdgcn_exp2f(a.x), m1 = __builtin_amdgcn_exp2f(a.y);
    const float f0 = __builtin_amdgcn_fractf(b.x), f1 = __builtin_amdgcn_fractf(b.y);
    o.r = f32x2{m0 * __builtin_amdgcn_cosf(f0), m1 * __builtin_amdgcn_cosf(f1)};
    o.i = f32x2{m0 * __builtin_amdgcn_sinf(f0), m1 * __builtin_amdgcn_sinf(f1)};
    return o;
}
constexpr unsigned BW_STRIDE = 40720, KEXP_RHO = 1216, KEXP_CH = 4 * KEXP_RHO, U_CH = 4160;
constexpr unsigned BW_K1 = 0, BW_U = KEXP_CH, BW_STG = BW_U + 2 * U_CH, STG_CH = 53 * 16, BW_END = BW_STG + 32 * STG_CH;
constexpr unsigned BW_K2 = BW_END - KEXP_CH, BW_C0 = BW_STG, BW_C1 = BW_C0 + 10240, BW_TAB = BW_C1 + 10240, BW_Y = 0;
static_assert(BW_END <= BW_STRIDE && BW_TAB + 1536 <= BW_K2, "role B LDS map");
DI void put_row(unsigned a, int row, cp2 v) { lds_st<v2u>(a + row * 16, v2u{pk2v(v.r), pk2v(v.i)}); }
DI cp2 csqr2(cp2 a) { const f32x2 t = a.r * a.i; return {a.r * a.r - a.i * a.i, t + t}; }

struct KgState { cp2 w, e1, e2, e4, e256; };
struct KgHead { float dt; f32x2 zr2, zi2; cp2 e8; };
DI KgHead kg_head(const KgIn& in) {
    KgHead h;
    h.dt = rbf(__expf(bflo(in.ldt)));
    const f32x2 zr = f32x2{bflo(in.are), bfhi(in.are)} * h.dt, zi = f32x2{bflo(in.aim), bfhi(in.aim)} * h.dt;
    h.zr2 = zr * 1.44269504088896341f; h.zi2 = zi * 0.15915494309189535f;
    h.e8 = cexp2(h.zr2, h.zi2, 8.f);
    return h;
}
DI float vmul(float a, float b) { float d; asm volatile("v_mul_f32 %0, %1, %2" : "=v"(d) : "v"(a), "v"(b)); return d; }
template <int S> DI void kg_head_step(const KgIn& in, KgHead& h, float (&t)[8]) {
    if constexpr (S == 0) { t[0] = bflo(in.ldt) * 1.44269504088896341f; PIN1(t[0]); t[0] = __builtin_amdgcn_exp2f(t[0]); PIN1(t[0]); }
    if constexpr (S == 1) { h.dt = rbf(t[0]); t[0] = bflo(in.are); t[1] = bfhi(in.are); PIN2(t[0], t[1]); PIN1(h.dt); }
    if constexpr (S == 2) { t[2] = bflo(in.aim); t[3] = bfhi(in.aim); PIN2(t[2], t[3]); t[0] = vmul(t[0], h.dt); t[1] = vmul(t[1], h.dt); }
    if constexpr (S == 3) { t[2] = vmul(t[2], h.dt); t[3] = vmul(t[3], h.dt); t[4] = 1.44269504088896341f; t[5] = 0.15915494309189535f; PIN2(t[4], t[5]); }
    if constexpr (S == 4) { h.zr2.x = vmul(t[0], t[4]); h.zr2.y = vmul(t[1], t[4]); h.zi2.x = vmul(t[2], t[5]); h.zi2.y = vmul(t[3], t[5]); }
    if constexpr (S == 5) { t[4] = 8.f; PIN1(t[4]); t[0] = vmul(h.zr2.x, t[4]); t[1] = vmul(h.zr2.y, t[4]); t[2] = vmul(h.zi2.x, t[4]); }
    if constexpr (S == 6) { t[3] = vmul(h.zi2.y, t[4]); t[2] = __builtin_amdgcn_fractf(t[2]); t[0] = __builtin_amdgcn_exp2f(t[0]); PIN2(t[0], t[2]); }
    if constexpr (S == 7) { t[3] = __builtin_amdgcn_fractf(t[3]); t[1] = __builtin_amdgcn_exp2f(t[1]); PIN2(t[1], t[3]); }
    if constexpr (S == 8) { t[4] = __builtin_amdgcn_cosf(t[2]); t[5] = __builtin_amdgcn_sinf(t[2]); PIN2(t[4], t[5]); }
    if constexpr (S == 9) { t[6] = __builtin_amdgcn_cosf(t[3]); t[7] = __builtin_amdgcn_sinf(t[3]); PIN2(t[6], t[7]); }
    if constexpr (S == 10) { h.e8.r.x = vmul(t[0], t[4]); h.e8.i.x = vmul(t[0], t[5]); h.e8.r.y = vmul(t[1], t[6]); h.e8.i.y = vmul(t[1], t[7]); }
}
template <class F>
DI KgState kg_lo(const KgIn& in, const KgHead& hd, unsigned sa, F&& mid) {
    KgState s;
    const float dt = hd.dt;
    const f32x2 zr2 = hd.zr2, zi2 = hd.zi2;
    const cp2 e8 = hd.e8;
    cp2 v0 = {f32x2{1.f, 1.f}, f32x2{0.f, 0.f}}, v1 = e8, v2 = csqr2(e8), v3 = cmul2(v2, e8);
    const cp2 e32 = csqr2(v2);
    put_row(sa, 21, v0); put_row(sa, 22, v1); put_row(sa, 23, v2); put_row(sa, 24, v3);
    v0 = e32;
    SB(); mid(IC<0>{}); SB();
    sfor<1, 8>([&](auto K) {
        constexpr int k = K.v;
        if constexpr (k > 1) v0 = cmul2(v0, e32);
        v1 = cmul2(v1, e32); v2 = cmul2(v2, e32); v3 = cmul2(v3, e32);
        put_row(sa, 21 + 4 * k, v0); put_row(sa, 22 + 4 * k, v1); put_row(sa, 23 + 4 * k, v2); put_row(sa, 24 + 4 * k, v3);
        SB(); mid(K); SB();
    });
    s.e256 = cmul2(v0, e32); s.e256.i = -s.e256.i;
    const cp2 db = {f32x2{bflo(in.bre), bfhi(in.bre)} * dt, f32x2{bflo(in.bim), bfhi(in.bim)} * dt};
    const cp2 cc = {f32x2{bflo(in.cre), bfhi(in.cre)}, f32x2{bflo(in.cim), bfhi(in.cim)}};
    s.w = cmul2(cc, db); s.w.i = -s.w.i;
    s.e1 = cexp2(zr2, -zi2, 1.f); s.e2 = csqr2(s.e1); s.e4 = csqr2(s.e2);
    return s;
}
constexpr int HSRC[21] = {0, 0, 0, 1, 0, 1, 2, 3, 0, 8, 8, 9, 8, 12, 12, 14, 14, 16, 17, 18, 19};
constexpr int HMUL[21] = {0, 0, 1, 1, 2, 2, 2, 2, 3, 1, 2, 2, 3, 2, 3, 2, 3, 3, 3, 3, 3};
DI const cp2& hmul(const KgState& s, int k) { return k == 0 ? s.e1 : k == 1 ? s.e2 : k == 2 ? s.e4 : s.e256; }
template <class F>
DI void kg_hi(const KgState& s, unsigned sa, F&& mid) {
    cp2 v[21];
    v[0] = s.w;
    sfor<0, 21>([&](auto R) {
        constexpr int r = R.v;
        if constexpr (r > 0) v[r] = cmul2(v[HSRC[r]], hmul(s, HMUL[r]));
        if constexpr (r & 1) { put_row(sa, r - 1, v[r - 1]); put_row(sa, r, v[r]); SB(); mid(R); SB(); }
        if constexpr (r == 20) put_row(sa, r, v[r]);
    });
}
struct KgAddr { unsigned ra, t0, t1, t2, t3, tb, wa; unsigned lag[16]; };
DI unsigned tab_addr(const KgAddr& k, int n) {
    unsigned d;
    if (n & 1) asm("v_mad_u32_u16 %0, %1, 2, %2 op_sel:[1,0,0,0]" : "=v"(d) : "v"(k.lag[n >> 1]), "v"(k.tb));
    else asm("v_mad_u32_u16 %0, %1, 2, %2" : "=v"(d) : "v"(k.lag[n >> 1]), "v"(k.tb));
    return d;
}
DI void kg_scatter(const KgAddr& k, const f32x16& d, int part) {
    if (part == 0) lds_st<v2u>(k.t0, v2u{pk2(d[0], d[1]), pk2(d[2], d[3])});
    if (part == 1) { lds_st<unsigned>(k.t1, pk2(d[4], d[5])); lds_st<unsigned>(k.t2, pk2(d[6], d[7])); }
    if (part == 2) { const unsigned p = pk2(d[8], d[9]); lds_st<u16>(k.t3, (u16)p); lds_st<u16>(k.t3 + 64, (u16)(p >> 16)); }
    if (part == 3) { const unsigned p = pk2(d[10], d[11]); lds_st<u16>(k.t3 + 128, (u16)p); lds_st<u16>(k.t3 + 192, (u16)(p >> 16)); }
}
template <class F>
DI void kg_product(const KgAddr& k, unsigned (&t)[32], F&& fill) {
    f32x16 d;
    v4u a[2], b[2];
#pragma unroll
    for (int q = 0; q < 2; ++q) { a[q] = lds_ld<v4u>(k.ra + q * 2 * STG_CH); b[q] = lds_ld<v4u>(k.ra + q * 2 * STG_CH + 21 * 16); }
    sfor<0, 16>([&](auto KS) {
        constexpr int ks = KS.v;
        d = mfma32<ks == 0>(a[ks & 1], b[ks & 1], d);
        SB();
        if constexpr (ks < 14) { a[ks & 1] = lds_ld<v4u>(k.ra + (ks + 2) * 2 * STG_CH); b[ks & 1] = lds_ld<v4u>(k.ra + (ks + 2) * 2 * STG_CH + 21 * 16); }
        fill(KS);
        SB();
    });
    unsigned ad[32];
#pragma unroll
    for (int n = 0; n < 32; ++n) { ad[n] = tab_addr(k, n); PIN1(ad[n]); }
    asm volatile("" : "+v"(d));
#pragma unroll
    for (int q = 0; q < 4; ++q) kg_scatter(k, d, q);
#pragma unroll
    for (int n = 0; n < 32; ++n) t[n] = lds_ld<u16>(ad[n]);
    SB();
}
DI void kg_expand(const KgAddr& k, unsigned koff, const unsigned (&t)[32], int r) {
    lds_st<v4u>(k.wa + koff + r * 256, v4u{t[8 * r] | t[8 * r + 1] << 16, t[8 * r + 2] | t[8 * r + 3] << 16, t[8 * r + 4] | t[8 * r + 5] << 16, t[8 * r + 6] | t[8 * r + 7] << 16});
}

DI float shadd(float prev, float cur, float y) {
    float d;
    asm volatile("v_add_f32_dpp %0, %1, %3 wave_ror:1 row_mask:0xf bank_mask:0xf\n\t"
                 "v_add_f32_dpp %0, %2, %3 wave_shr:1 row_mask:0xf bank_mask:0xf" : "=&v"(d) : "v"(prev), "v"(cur), "v"(y));
    return d;
}
DI float shadd0(float cur, float y) {
    float d;
    asm volatile("v_add_f32_dpp %0, %1, %2 wave_shr:1 row_mask:0xf bank_mask:0xf bound_ctrl:0" : "=&v"(d) : "v"(cur), "v"(y));
    return d;
}

constexpr int epi_upto(int i, int dl) {
    int e = 0;
    for (int a = 1; a <= i; ++a)
        for (int b = 0; b < 8 - a && (a < i || b <= dl); ++b) {
            const int lim = 12 * (a <= 4 ? a : a - 1);
            const int n = (b == 7 - a) ? 1 : 2;
            e = e + n > lim ? lim : e + n;
        }
    return e;
}
struct Conv {
    unsigned aA, aB;
    float p2[32], p1[32], y[32];
    DI void init(unsigned kexp, unsigned ub, int lane) {
        const int hh = lane >> 5;
        aA = kexp + (lane & 3) * KEXP_RHO + (56 - 8 * ((lane & 31) >> 2) + 8 * hh) * 2;
        aB = ub + 2 * (4 + 32 * hh + 4 * ((lane >> 2) & 3) + 16 * ((lane >> 4) & 1) + 4 * (lane & 3));
    }
    DI v4u ldA(int c, int dl) const { return lds_ld<v4u>(aA + (64 * (7 - dl) + 16 * c) * 2); }
    DI v4u ldB(int c, int i) const { const v2u x = lds_tr(aB + 128 * (4 * i + c)), z = lds_tr(aB + 128 * (4 * i + c) + 32); return v4u{x.x, x.y, z.x, z.y}; }
    template <int e> DI void epi(const f32x16& t, float last3) {
        constexpr int G = 4 * (e / 12) + (e & 3), st = (e % 12) >> 2, g = G & 3;
        const float yy = t[4 * g + 2 - st];
        if constexpr (st == 0) {
            if constexpr (G == 0) p2[0] = shadd0(t[3], yy);
            else p2[G] = shadd(g ? t[4 * g - 1] : last3, t[4 * g + 3], yy);
        } else if constexpr (st == 1) {
            if constexpr (G == 0) p1[0] = shadd0(p2[0], yy); else p1[G] = shadd(p2[G - 1], p2[G], yy);
        } else {
            if constexpr (G == 0) y[0] = shadd0(p1[0], yy); else y[G] = shadd(p1[G - 1], p1[G], yy);
        }
    }
};
template <class F>
DI void conv_first(Conv& cv, f32x16 (&acc)[8], F&& fill) {
    v4u af[2][8];
#pragma unroll
    for (int dl = 0; dl < 8; ++dl) af[0][dl] = cv.ldA(0, dl);
    v4u b0 = cv.ldB(0, 0), b1 = cv.ldB(0, 1);
    sfor<0, 32>([&](auto CI) {
        constexpr int c = CI.v >> 3, i = CI.v & 7;
        const v4u bf = b0;
        b0 = b1;
        constexpr int ni = (i + 2) & 7, nc = c + (i >= 6);
        if constexpr (nc < 4) b1 = cv.ldB(nc, ni);
        if constexpr (c < 3) af[(c + 1) & 1][i] = cv.ldA(c + 1, i);
        SB();
        sfor<0, 8 - i>([&](auto DL) {
            constexpr int dl = DL.v, m = 36 * c + 8 * i - i * (i - 1) / 2 + dl;
            acc[i + dl] = mfma32<c == 0 && i == 0>(af[c & 1][dl], bf, acc[i + dl]);
            SB();
            fill(IC<m>{}, IC<(dl == 7 - i) ? -1 : m - CI.v>{});
            if constexpr (c == 3 && i > 0)
                sfor<(dl ? epi_upto(i, dl - 1) : epi_upto(i - 1, 8 - i)), epi_upto(i, dl)>([&](auto E) { cv.template epi<E.v>(acc[E.v / 12], acc[E.v / 12 ? E.v / 12 - 1 : 0][15]); });
            SB();
        });
    });
}
static_assert(epi_upto(7, 0) == 48, "tiles 0..3 of the first channel are folded inside conv_first");
struct Sched2 {
    int c[144], t[144], i[144], rd[144], slot[48 + 8 * 15], order[48 + 8 * 15], start[146];
    constexpr Sched2() : c{}, t{}, i{}, rd{}, slot{}, order{}, start{} {
        int m = 0, done[8] = {};
        for (int cc = 0; cc < 4; ++cc)
            for (int ii = 0; ii < 4; ++ii)
                for (int tt = ii; tt < 4; ++tt, ++m) { c[m] = cc; t[m] = tt; i[m] = ii; rd[m] = tt == 3; }
        for (int cc = 0; cc < 3; ++cc)
            for (int ii = 0; ii < 8; ++ii)
                for (int tt = ii > 4 ? ii : 4; tt < 8; ++tt, ++m) { c[m] = cc; t[m] = tt; i[m] = ii; rd[m] = tt == 7; }
        for (int tt = 4; tt < 8; ++tt)
            for (int ii = 0; ii <= tt; ++ii, ++m) { c[m] = 3; t[m] = tt; i[m] = ii; rd[m] = 0; }
        for (int k = 0; k < 144; ++k) done[t[k]] = k;
        int cur = 3, used = 0;
        for (int o = 0; o < 48 + 8 * 15; ++o) {
            const int tile = o < 48 ? -1 : (o - 48) / 15, kind = o < 48 ? 0 : (o - 48) % 15;
            int ear = tile < 0 ? 3 : done[tile] + 4;
            if (kind == 14) ear = slot[o - 1] + 3;
            const int wgt = kind == 12 ? 4 : kind > 12 ? 1 : 2;
            if (kind == 14) { slot[o] = ear < 144 ? ear : 144; continue; }
            if (cur < ear) { cur = ear; used = 0; }
            while (cur < 144 && used + wgt > (rd[cur] ? 2 : 4) && used > 0) { ++cur; used = 0; }
            slot[o] = cur < 144 ? cur : 144;
            used += wgt;
        }
        int n = 0;
        for (int k = 0; k <= 144; ++k) {
            start[k] = n;
            for (int o = 0; o < 48 + 8 * 15; ++o) if (slot[o] == k) order[n++] = o;
        }
        start[145] = n;
    }
};
constexpr Sched2 S2T{};
constexpr int NOPS2 = 48 + 8 * 15;

__global__ __launch_bounds__(256, 1) void k_fused(Params p) {
    const int tid = threadIdx.x, lane = tid & 63;
    const int w = __builtin_amdgcn_readfirstlane(tid >> 6);
    const int b = blockIdx.x, xl = b & 7, r = b >> 3;
    STAMP(0);
    if (w == 3) {
        u64 pc; unsigned len;
        asm volatile("s_getpc_b64 %0\n.Lkf_pc:\n\ts_mov_b32 %1, .Lkf_end_a-.Lkf_pc\n\ts_max_i32 %1, %1, .Lkf_end_b-.Lkf_pc\n\t"
                     "s_max_i32 %1, %1, .Lkf_end_g-.Lkf_pc" : "=s"(pc), "=&s"(len)::"scc");
        touch((r * 20 + lane) * 128 + (lane < 20 ? 0 : 0x40000000), mk((const void*)pc, len), 0);
    }
    const rsrc_t ws = mk(p.ws, WS_END);
    const rsrc_t rx = mk(p.x, XBYTES);
    unsigned tA, tB;
    constexpr unsigned BW_PAR = BW_U + U_CH;
    constexpr unsigned A_PAR = 131072;
    const int c = 4 * (xl & 3) + (r & 3), g = 5 * (xl >> 2) + (r >> 2);
    const rsrc_t rbre = mk(p.bre, MBYTES), rbim = mk(p.bim, MBYTES);
    const unsigned gch = ((lane & 7) ^ ((4 * w + (lane >> 4)) & 7)) * 16;
    const unsigned vox = (32 * (lane >> 5) + ((lane >> 3) & 3)) * 1280 + gch, vob = (lane >> 3) * 1280 + gch;
    const unsigned sox = (128 * c + 4 * w) * 1280, sob = (64 * g + 8 * w) * 1280;
    auto dma_piece = [&](int ti, unsigned slot, unsigned ko) {
        const unsigned l = slot + ti * 4096 + w * 1024;
        if (ti < 4) dma16(l, vox, rx, sox + (64 * (ti >> 1) + 16 * (ti & 1)) * 1280 + ko);
        else dma16(l, vob, (ti & 1) ? rbim : rbre, sob + 32 * ((ti - 4) >> 1) * 1280 + ko);
    };
    if (r < 20) {
        const void* const pp[3] = {p.lre, p.lim, p.ldt};
#pragma unroll
        for (int k = 0; k < 3; ++k)
            asm volatile("s_mov_b32 m0, %0\n\ts_nop 0\n\tbuffer_load_dword %1, %2, %3 offen lds" ::"s"(A_PAR + w * 1024 + k * 256),
                         "v"(lane * 4), "s"(mk(pp[k], VBYTES)), "s"((64 * g + 32 * (w >> 1)) * 2) : "memory");
        asm volatile("s_mov_b32 m0, %0\n\ts_nop 0\n\tbuffer_load_dword %1, %2, 0 offen sc1 lds" ::"s"(A_PAR + w * 1024 + 768),
                     "v"(lane < 2 ? lane * 4 : 0x40000000), "s"(ws) : "memory");
#pragma unroll
        for (int ti = 0; ti < 8; ++ti) dma_piece(ti, 0, 0);
        if (w < 2) TOUCH((lane & 31) * 1280 + (lane >> 5) * 128, mk((r & 2) ? p.bim : p.bre, MBYTES), w ? 0x40000000 : (64 * g + 32 * (r & 1)) * 1280 + 128);
        else TOUCH((lane & 31) < 26 ? (lane & 31) * 1280 + (lane >> 5) * 128 : 0x40000000, rx, w == 3 ? 0x40000000 : (128 * c + ((r >> 2) * 51 >> 1)) * 1280 + 128);
    } else if (r < 30) {
        const int jj = r - 20, grp = xl * 10 + jj, tb = 2 * (xl & 3) + (xl >> 2);
        const unsigned bw = w * BW_STRIDE, po = (grp * 8 + 2 * w) * 256;
        const void* const pp[6] = {p.sare, p.saim, p.sbre, p.sbim, p.scre, p.scim};
#pragma unroll
        for (int k = 0; k < 6; ++k)
            asm volatile("s_mov_b32 m0, %0\n\ts_nop 0\n\tbuffer_load_dwordx4 %1, %2, %3 offen nt lds" ::"s"(bw + BW_PAR + k * 1024),
                         "v"(lane < 32 ? lane * 16 : 0x40000000), "s"(mk(pp[k], SBYTES)), "s"(po) : "memory");
        asm volatile("s_mov_b32 m0, %0\n\ts_nop 0\n\tbuffer_load_dword %1, %2, 0 offen sc1 lds" ::"s"(bw + BW_PAR + 6144),
                     "v"(lane < 2 ? lane * 4 : 0x40000000), "s"(ws) : "memory");
        const int rb = lane >> 3;
#pragma unroll
        for (int i = 0; i < 8; ++i) {
            const unsigned f = (((rb >> 1) & 1) | ((i & 1) << 1)) << 1;
            dma16(bw + i * 1024, rb * 1280 + (((lane & 7) ^ f) * 16), rx, (256 * tb + 64 * w + 8 * i) * 1280 + jj * 128);
        }
    }
    int role = r;
    asm volatile("" : "+s"(role));

    if (__builtin_expect(role < 20, 1)) {
        const int th = w & 1, cg = w >> 1, ch = lane & 31, hh = lane >> 5;
        const int pch = 64 * g + 32 * cg + ch;
        STAMP(11);
        const int c2 = 2 * (2 * (xl & 3) + (xl >> 2)) + (r & 1), ct = r >> 1;
        CStage cs;
        cs.r0 = mk((w & 1) ? p.cim : p.cre, MBYTES); cs.r1 = mk((w & 1) ? p.cre : p.cim, MBYTES); cs.w = w;
#pragma unroll
        for (int e = 0; e < 2; ++e) {
            cs.vo[e] = (64 * ct + (lane >> 3)) * 1280 + (((lane & 7) ^ ((4 * e + (lane >> 4)) & 7)) * 16);
            cs.ra[e] = (lane & 15) * 128 + (((4 * e + (lane >> 4)) ^ ((lane >> 1) & 7)) * 16);
        }
        v4u cf[40];
        const int rot = 2 * (w + ct), own = rot & 7, flip = FLIP ? (ct >> 2) & 1 : 0, c4 = lane & 3;
        const unsigned eb = 135168 + w * 5120;
        auto e_dma = [&](int i) {
            if (i < 4) dma16(eb + i * 1024, (lane >> 3) * 1280 + (((lane & 7) ^ (((lane >> 4) & 3) * 2)) * 16), rx, (128 * c2 + 16 * own + 8 * i) * 1280 + ct * 128);
            else asm volatile("s_mov_b32 m0, %0\n\ts_nop 0\n\tbuffer_load_dword %1, %2, %3 offen lds" ::"s"(eb + 4096 + (i - 4) * 256),
                              "v"(lane * 4), "s"(mk(i == 4 ? p.dska : p.dskb, VBYTES)), "s"(ct * 128) : "memory");
        };
        const int seg = 2 * th + hh;
        const unsigned hs = w * 8192;
        unsigned wbx = hs + ch * 128 + (((8 * hh) ^ (((ch >> 1) & 1) << 2 | ((ch >> 3) & 1) << 3 | (ch & 1) | ((ch >> 2) & 1) << 1)) * 8);
        unsigned rad[2];
#pragma unroll
        for (int hf = 0; hf < 2; ++hf) {
            const int row = 8 * (lane >> 4) + 4 * hf + ((lane >> 2) & 3);
            const unsigned gs = ((row >> 1) & 1) << 2 | ((row >> 3) & 1) << 3 | (row & 1) | ((row >> 2) & 1) << 1;
            rad[hf] = hs + row * 128 + (((lane & 3) ^ gs) * 8);
        }
        unsigned wa[8], ra8[8];
#pragma unroll
        for (int q = 0; q < 8; ++q) { wa[q] = wbx ^ (q * 8); ra8[q] = rad[q & 1] ^ ((q >> 1) * 32); PIN2(wa[q], ra8[q]); }
        unsigned gof[4];
#pragma unroll
        for (int i = 0; i < 4; ++i) {
            const int j = c - 1 - (4 * i + seg);
            gof[i] = j < 0 ? WS_END : WS_AGG + (j * 640 + pch) * 16;
            PIN1(gof[i]);
        }
        int tok = 128 * c2 + 16 * own + (lane >> 2);
        PIN1(tok);
        const unsigned xch = 159744 + (cg * 2) * 256 + ch * 8;
        unsigned aggo = (hh == 0 && th == 1 && c < 15) ? WS_AGG + (c * 640 + pch) * 16 : WS_END;
        unsigned xchw = xch + th * 256;
        PIN2(aggo, xchw);
        unsigned adA[4], adB[4];
        {
            const unsigned lb = ch * 128 + ((hh ^ ((ch >> 1) & 7)) * 16);
#pragma unroll
            for (int k = 0; k < 4; ++k) { adA[k] = (lb ^ (k * 32)) + th * 8192; adB[k] = (lb ^ (k * 32)) + 16384 + cg * 8192; }
        }
#pragma unroll
        for (int k = 0; k < 4; ++k) PIN2(adA[k], adB[k]);
        PIN2(cs.vo[0], cs.vo[1]); PIN2(cs.ra[0], cs.ra[1]);
        unsigned gfo = WS_FLAG + (FL_G + c2) * 256;
        PIN1(gfo);
        WAITV(APF == 1 ? 10 : APF == 2 ? 9 : 8);
        const unsigned vlr = lds_ld<u16>(A_PAR + w * 1024 + ch * 2), vli = lds_ld<u16>(A_PAR + w * 1024 + 256 + ch * 2),
                       vld = lds_ld<u16>(A_PAR + w * 1024 + 512 + ch * 2);
        {
            const v2u ep = lds_ld<v2u>(A_PAR + w * 1024 + 768);
            tA = rfl(ep.x) + 1; tB = rfl(ep.y) + 1;
        }
        cpx lam, S2;
        {
            const float dt = rbf(__expf(bflo(vld))), Lr = bflo(vlr), Li = bflo(vli);
            const float e = __expf(dt * Lr), fr = __builtin_amdgcn_fractf((dt * Li) * 0.15915494309189535f);
            lam = {e * __builtin_amdgcn_cosf(fr), e * __builtin_amdgcn_sinf(fr)};
            const float nr = lam.r - 1.f, ni = lam.i, den = 2.f * __builtin_amdgcn_rcpf(Lr * Lr + Li * Li);
            S2 = {(nr * Lr + ni * Li) * den, (ni * Lr - nr * Li) * den};
        }
        PIN2(lam.r, lam.i); PIN2(S2.r, S2.i);
        float qr[32], qi[32];
        cpx pw[10];
        qr[0] = 1.f; qi[0] = 0.f; qr[1] = lam.r; qi[1] = lam.i;
        cpx sel0, sel1, sel2, sel3;
        auto tbl = [&](auto H) {
            constexpr int h = H.v, k = h >> 1;
            cpx a, bq, d;
            if constexpr (k < 30) {
                constexpr int n = k + 2, hb = n < 4 ? 2 : n < 8 ? 4 : n < 16 ? 8 : 16;
                constexpr int ia = n == hb ? hb / 2 : n - hb, ib = n == hb ? hb / 2 : hb;
                a = {qr[ia], qi[ia]}; bq = {qr[ib], qi[ib]};
                if constexpr (h & 1) d = {qr[n], qi[n]};
            } else {
                if constexpr (h == 70) {
                    sel0 = hh ? pw[0] : cpx{1.f, 0.f}; sel1 = th ? pw[1] : cpx{1.f, 0.f};
                    sel2 = hh ? pw[2] : cpx{1.f, 0.f}; sel3 = th ? pw[3] : cpx{1.f, 0.f};
                }
                if constexpr (k == 30) a = bq = {qr[16], qi[16]};
                else if constexpr (k < 35) a = bq = pw[k - 31];
                else if constexpr (k == 35) { a = sel0; bq = sel1; }
                else if constexpr (k == 36) { a = sel2; bq = sel3; }
                else { a = S2; bq = lam; }
                if constexpr (h & 1) d = pw[k - 30];
            }
            if constexpr (h & 1) CMUL_B(d.r, d.i, a.i, bq.r, bq.i); else CMUL_A(d.r, d.i, a.r, bq.r, bq.i);
            if constexpr (k < 30) { qr[k + 2] = d.r; qi[k + 2] = d.i; } else pw[k - 30] = d;
        };

        f32x16 aR[2], aI[2];
        STAMP(1);
#pragma unroll
        for (int ti = 0; ti < 8; ++ti) dma_piece(ti, 32768, 128);
        asm volatile("s_waitcnt vmcnt(%0)\n\ts_barrier" ::"n"(APF == 2 ? 9 : 8) : "memory");
        STAMP(10);
        v4u fa0 = lds_ld<v4u>(adA[0]), fa1 = lds_ld<v4u>(adA[0] + 4096), fr = lds_ld<v4u>(adB[0]), fi = lds_ld<v4u>(adB[0] + 4096);
        const unsigned cb0 = 98304 + w * 8192, cb1 = 65536 + w * 8192;
        sfor<0, 40>([&](auto SK) {
            constexpr int s = SK.v >> 2, k = SK.v & 3;
            v4u na0 = fa0, na1 = fa1, nr = fr, ni = fi;
            if constexpr (k == 3 && s <= 8) asm volatile("s_waitcnt vmcnt(6) lgkmcnt(0)\n\ts_barrier" ::: "memory");
            constexpr int kn = (k + 1) & 3;
            constexpr unsigned so = ((k == 3 ? s + 1 : s) % 3) * 32768;
            constexpr bool rd = SK.v < 39;
            auto req = [&](auto N) {
                if constexpr (s <= 7) dma_piece(N.v, ((s + 2) % 3) * 32768, (s + 2) * 128);
                else if constexpr (s == 8) c_dma(cs, 0, N.v, cb0);
                else if constexpr (N.v < 6) e_dma(N.v);
            };
            aR[0] = mfma32<SK.v == 0>(fa0, fr, aR[0]); SB();
            if constexpr (rd) { na0 = lds_ld<v4u>(adA[kn] + so); nr = lds_ld<v4u>(adB[kn] + so); }
            if constexpr (2 * SK.v < 76) tbl(IC<2 * SK.v>{});
            SB();
            aI[0] = mfma32<SK.v == 0>(fa0, fi, aI[0]); SB();
            if constexpr (rd) { ni = lds_ld<v4u>(adB[kn] + so + 4096); na1 = lds_ld<v4u>(adA[kn] + so + 4096); }
            if constexpr (2 * SK.v + 1 < 76) tbl(IC<2 * SK.v + 1>{});
            SB();
            aR[1] = mfma32<SK.v == 0>(fa1, fr, aR[1]);
            req(IC<2 * k>{});
            aI[1] = mfma32<SK.v == 0>(fa1, fi, aI[1]);
            req(IC<2 * k + 1>{});
            SB();
            fa0 = na0; fa1 = na1; fr = nr; fi = ni;
        });
        f32x2 Qr[16], Qi[16];
#pragma unroll
        for (int i = 0; i < 16; ++i) { Qr[i] = f32x2{qr[2 * i], qr[2 * i + 1]}; Qi[i] = f32x2{qi[2 * i], qi[2 * i + 1]}; PIN2(Qr[i], Qi[i]); }
        asm volatile("s_nop 11" : "+v"(aR[0]), "+v"(aI[0]), "+v"(aR[1]), "+v"(aI[1]));
        STAMP(2);
        const cpx l32 = pw[0], l64 = pw[1], l512 = pw[4], lseg = pw[5], lgat = pw[6], Sl = pw[7];
        cpx agg;
        {
            f32x2 sr0 = {0.f, 0.f}, si0 = {0.f, 0.f}, sr1 = {0.f, 0.f}, si1 = {0.f, 0.f};
#pragma unroll
            for (int i = 0; i < 8; ++i) {
                const f32x2 ur = {aR[0][2 * i], aR[0][2 * i + 1]}, ui = {aI[0][2 * i], aI[0][2 * i + 1]};
                const f32x2 q0 = Qr[15 - i].yx, q1 = Qi[15 - i].yx;
                sr0 += q0 * ur; si0 += q0 * ui; sr0 -= q1 * ui; si0 += q1 * ur;
                const f32x2 vr = {aR[1][2 * i], aR[1][2 * i + 1]}, vi = {aI[1][2 * i], aI[1][2 * i + 1]};
                const f32x2 p0 = Qr[7 - i].yx, p1 = Qi[7 - i].yx;
                sr1 += p0 * vr; si1 += p0 * vi; sr1 -= p1 * vi; si1 += p1 * vr;
            }
            sr0 += sr1; si0 += si1;
            agg = {sr0.x + sr0.y, si0.x + si0.y};
        }
        cpx a0, a1;
        halves(agg.r, a0.r, a1.r); halves(agg.i, a0.i, a1.i);
        const cpx a64 = cfma(l32, a0, a1);
        lds_st<f32x2>(xchw, f32x2{a64.r, a64.i});
        BARRIER();
        const f32x2 o64v = lds_ld<f32x2>(xch);
        const cpx o64 = {o64v.x, o64v.y};
        {
            const cpx a128 = cfma(l64, o64, a64);
            __builtin_amdgcn_raw_buffer_store_b128(v4u{__float_as_uint(a128.r), tA, __float_as_uint(a128.i), tB}, ws, aggo, 0, AUX_SC1);
        }
        SB();
        STAMP(3);
        cpx pre;
        {
            const cpx z = {th ? o64.r : 0.f, th ? o64.i : 0.f}, t = cfma(l32, z, a0);
            pre = {hh ? t.r : z.r, hh ? t.i : z.i};
        }
        PIN2(pre.r, pre.i);
        {
            const float nli = -lam.i;
#pragma unroll
            for (int t = 1; t < 32; ++t) {
                float xr = aR[t >> 4][t & 15], xi = aI[t >> 4][t & 15];
                asm volatile("v_fmac_f32 %0, %4, %2\n\tv_fmac_f32 %1, %4, %3\n\tv_fmac_f32 %0, %6, %3\n\tv_fmac_f32 %1, %5, %2"
                             : "+v"(xr), "+v"(xi) : "v"(aR[(t - 1) >> 4][(t - 1) & 15]), "v"(aI[(t - 1) >> 4][(t - 1) & 15]), "v"(lam.r), "v"(lam.i), "v"(nli));
                aR[t >> 4][t & 15] = xr; aI[t >> 4][t & 15] = xi;
                if (t % 4 == 1) c_dma(cs, 1, t / 4, cb1);
            }
            WAITV(9);
#pragma unroll
            for (int f = 0; f < 8; ++f) c_read(cs, cf, 0, f, cb0);
            SB();
#pragma unroll
            for (int i = 0; i < 16; ++i) {
                const int t = 2 * i;
                const f32x2 gr = {aR[t >> 4][t & 15], aR[t >> 4][(t & 15) + 1]}, gi = {aI[t >> 4][t & 15], aI[t >> 4][(t & 15) + 1]};
                f32x2 hr = gr * S2.r - gi * S2.i, hi = -gi * S2.r - gr * S2.i;
                PIN2(hr, hi);
                aR[t >> 4][t & 15] = hr.x; aR[t >> 4][(t & 15) + 1] = hr.y; aI[t >> 4][t & 15] = hi.x; aI[t >> 4][(t & 15) + 1] = hi.y;
            }
        }
        v4u ga[4]; v2u gfl;
        asm volatile("buffer_load_dwordx4 %0, %5, %10, 0 offen sc1\n\tbuffer_load_dwordx4 %1, %6, %10, 0 offen sc1\n\t"
                     "buffer_load_dwordx4 %2, %7, %10, 0 offen sc1\n\tbuffer_load_dwordx4 %3, %8, %10, 0 offen sc1\n\t"
                     "buffer_load_dwordx2 %4, %9, %10, 0 offen sc1\n\ts_waitcnt lgkmcnt(0)"
                     : "=&v"(ga[0]), "=&v"(ga[1]), "=&v"(ga[2]), "=&v"(ga[3]), "=&v"(gfl)
                     : "v"(gof[0]), "v"(gof[1]), "v"(gof[2]), "v"(gof[3]), "v"(gfo), "s"(ws) : "memory");
#pragma unroll
        for (int i = 0; i < 8; ++i) c_dma(cs, 2, i, cb0);
        asm volatile("s_waitcnt vmcnt(8)" : "+v"(ga[0]), "+v"(ga[1]), "+v"(ga[2]), "+v"(ga[3]), "+v"(gfl)::"memory");
        cpx carry = {0.f, 0.f};
        if (c > 0) {
            auto all_here = [&]() {
                bool ok = true;
#pragma unroll
                for (int i = 0; i < 4; ++i) ok = ok & ((gof[i] == WS_END) | ((ga[i].y == tA) & (ga[i].w == tB)));
                return __all(ok);
            };
            if (!all_here()) {
#pragma unroll 1
                for (int spin = 0; spin < SPIN_CAP; ++spin) {
                    MEMFENCE();
#pragma unroll
                    for (int i = 0; i < 4; ++i) ga[i] = __builtin_amdgcn_raw_buffer_load_b128(ws, gof[i], 0, AUX_SC1);
                    if (all_here()) break;
                }
            }
            cpx part = {__uint_as_float(ga[3].x), __uint_as_float(ga[3].z)};
#pragma unroll
            for (int i = 2; i >= 0; --i) part = cfma(l512, part, cpx{__uint_as_float(ga[i].x), __uint_as_float(ga[i].z)});
            part = cmul(lgat, part);
            cpx p0, p1;
            halves(part.r, p0.r, p1.r); halves(part.i, p0.i, p1.i);
            const cpx ps = {p0.r + p1.r, p0.i + p1.i};
            lds_st<f32x2>(xchw + 1024, f32x2{ps.r, ps.i});
            BARRIER();
            const f32x2 o = lds_ld<f32x2>((xchw ^ 256) + 1024);
            carry = {ps.r + o.x, ps.i + o.y};
        }
        STAMP(4);
        if (!__all((gfl.x == tA) & (gfl.y == tB))) poll_flags(ws, gfo, tA, tB);
        __builtin_amdgcn_fence(__ATOMIC_ACQUIRE, "wavefront");
        MEMFENCE();
        float gate[2], omg[2];
#pragma unroll
        for (int e = 0; e < 2; ++e) gate[e] = __uint_as_float(__builtin_amdgcn_raw_buffer_load_b32(ws, WS_G + tok * 4, (e ^ flip) * 64, AUX_SC1));
        MEMFENCE();
        const cpx F = cmul(Sl, cfma(lseg, carry, pre));
        const unsigned cb2 = 32768 + w * 8192;
        const unsigned hso = WS_H + ((2 * c + th) * 160 + 4 * g + cg) * 1024;
#pragma unroll
        for (int hv = 0; hv < 2; ++hv) {
#pragma unroll
            for (int q = 4 * hv; q < 4 * hv + 4; ++q) {
                f32x2 hr[2], hi[2];
#pragma unroll
                for (int e = 0; e < 2; ++e) {
                    const int t = 4 * q + 2 * e;
                    const f32x2 ur = {aR[t >> 4][t & 15], aR[t >> 4][(t & 15) + 1]}, ui = {aI[t >> 4][t & 15], aI[t >> 4][(t & 15) + 1]};
                    const f32x2 q0 = Qr[t >> 1], q1 = Qi[t >> 1];
                    hr[e] = ur + q0 * F.r - q1 * F.i;
                    hi[e] = ui - q0 * F.i - q1 * F.r;
                }
                lds_st<v2u>(wa[q], v2u{pk2v(hr[0]), pk2v(hr[1])});
                lds_st<v2u>(wa[q] + 4096, v2u{pk2v(hi[0]), pk2v(hi[1])});
                SB();
                if (hv == 0) { c_dma(cs, 3, 2 * q, cb2); c_dma(cs, 3, 2 * q + 1, cb2); }
                SB();
            }
            v4u hq[4];
#pragma unroll
            for (int i = 0; i < 4; ++i) {
                const int mt = hv + 2 * (i & 1);
                const v2u x0 = lds_tr(ra8[2 * mt] + (i >> 1) * 4096), x1 = lds_tr(ra8[2 * mt + 1] + (i >> 1) * 4096);
                hq[i] = v4u{x0.x, x0.y, x1.x, x1.y};
            }
            SB();
#pragma unroll
            for (int i = 0; i < 4; ++i)
                __builtin_amdgcn_raw_buffer_store_b128(hq[i], ws, lane * 16, hso + (hv + 2 * (i & 1)) * 40960 + (i >> 1) * 2048, AUX_SC1);
            asm volatile("s_nop 1");
            SB();
        }
        STAMP(13);
        asm volatile("" : "+v"(gate[0]), "+v"(gate[1])::"memory");
#pragma unroll
        for (int f = 0; f < 8; ++f) { c_read(cs, cf, 1, f, cb1); c_read(cs, cf, 2, f, cb0); }
        float o[2][16];
        {
            const unsigned xa = (lane >> 2) * 128 + (c4 & 1) * 8, sw = ((lane >> 3) & 3) * 2;
#pragma unroll
            for (int e = 0; e < 2; ++e) omg[e] = rbf(1.f - gate[e]);
#pragma unroll
            for (int nt = 0; nt < 4; ++nt) {
                const v2u av = lds_ld<v2u>(eb + 4096 + nt * 32 + c4 * 8), bv = lds_ld<v2u>(eb + 4352 + nt * 32 + c4 * 8);
#pragma unroll
                for (int e = 0; e < 2; ++e) {
                    const v2u xv = lds_ld<v2u>((xa ^ ((e ^ flip) * 2048)) + eb + (((2 * nt + (c4 >> 1)) ^ sw) * 16));
#pragma unroll
                    for (int k = 0; k < 2; ++k) {
                        const unsigned x1 = xv[k], a1 = av[k], b1 = bv[k];
                        f32x2 t = f32x2{bflo(x1), bfhi(x1)} * (f32x2{bflo(a1), bfhi(a1)} * gate[e] + f32x2{bflo(b1), bfhi(b1)} * omg[e]);
                        PIN1(t);
                        o[e][4 * nt + 2 * k] = t.x; o[e][4 * nt + 2 * k + 1] = t.y;
                    }
                }
            }
        }
        const unsigned pwo = (lane & 15) * 64 + (((lane >> 4) ^ ((lane >> 1) & 3)) * 16);
        const unsigned pro = (lane >> 2) * 64 + (((lane & 3) ^ ((lane >> 3) & 3)) * 16);
        const unsigned priv = 98304 + w * 8192;
        unsigned so[8], pc[8], pr[3];
#pragma unroll
        for (int q = 0; q < 8; ++q) {
            const unsigned T = (rot + 2 + (q ^ flip)) & 7;
            so[q] = WS_H + ((8 * c2 + T) * 40 + 10 * w) * 1024;
            asm volatile("" : "+s"(so[q]));
            pc[q] = T * 4096 + lane * 16; PIN1(pc[q]);
        }
        pr[0] = priv + pwo; pr[1] = priv + pro;
        unsigned yfo = WS_FLAG + (FL_Y + ct * 8 + (lane & 7)) * 256;
        PIN1(yfo);
        unsigned ywa = priv + 4096 + (lane >> 3) * 128 + (((lane & 7) + 2 * ((lane >> 4) & 3)) & 7) * 16;
        pr[2] = priv + 4096 + c4 * 256 + (((lane >> 2) * 4 + 32 * c4) & 127);
        PIN2(pr[0], pr[1]); PIN2(pr[2], ywa);
        asm volatile("s_waitcnt vmcnt(0) lgkmcnt(0)\n\ts_barrier" ::: "memory");
        set_flag(ws, FL_H + c * 10 + g, tA, tB);
        STAMP(5);
        MEMFENCE();
#pragma unroll
        for (int i = 0; i < 8; ++i) c_dma(cs, 4, i, cb1);
#pragma unroll
        for (int f = 0; f < 8; ++f) c_read(cs, cf, 3, f, cb2);
        poll_flags(ws, WS_FLAG + (FL_H + c2 * 10 + ((10 * w) >> 2) + (lane < 3 ? lane : 0)) * 256, tA, tB);
        STAMP(6);
        constexpr int WIN = HWIN;
        v4u hv[WIN];
        unsigned hvo[3] = {(unsigned)lane * 16, (unsigned)lane * 16 + 4096, (unsigned)lane * 16 + 8192};
        PIN2(hvo[1], hvo[2]);
        auto h_load = [&](int i) { return __builtin_amdgcn_raw_buffer_load_b128(ws, hvo[(i % 10) >> 2] + ((i % 10) & 3) * 1024, so[i / 10], AUX_SC1); };
#pragma unroll
        for (int i = 0; i < WIN; ++i) { hv[i] = h_load(i); SB(); }
#pragma unroll
        for (int f = 0; f < 8; ++f) c_read(cs, cf, 4, f, cb1);
        SB();
        f32x4 acc[3][4];
        v2u yfl;
        v4u yq[4];
        bool yearly = false;
        auto y_load = [&]() {
            __builtin_amdgcn_fence(__ATOMIC_ACQUIRE, "wavefront");
            MEMFENCE();
#pragma unroll
            for (int i = 0; i < 4; ++i)
                yq[i] = __builtin_amdgcn_raw_buffer_load_b128(ws, (lane >> 3) * 8192 + (lane & 7) * 16,
                                                              WS_Y + (32 * ct + 8 * i) * 8192 + (128 * c2 + 16 * own) * 4, AUX_SC1);
        };
        auto add_part = [&](int e, int nt, const f32x4& v) {
#pragma unroll
            for (int k = 0; k < 4; ++k) asm volatile("v_fmac_f32 %0, %1, %2" : "+v"(o[e][4 * nt + k]) : "v"(gate[e]), "v"(v[k]));
        };
        sfor<0, 80>([&](auto I) {
            constexpr int i = I.v, q = i / 10, j = i % 10;
            const v4u hf = hv[i % WIN];
            sfor<0, 4>([&](auto NT) {
                constexpr int nt = NT.v;
                if constexpr (j == 0 && q < 2) acc[q % 3][nt] = mfma16z(cf[nt], hf); else acc[q % 3][nt] = mfma16(cf[4 * j + nt], hf, acc[q % 3][nt]);
                SB();
                if constexpr (nt == 0 && i >= 1 && i - 1 + WIN < 80) hv[(i - 1) % WIN] = h_load(i - 1 + WIN);
                if constexpr (nt == 2 && i == 60) yfl = __builtin_amdgcn_raw_buffer_load_b64(ws, yfo, 0, AUX_SC1);
                if constexpr (nt == 1 && q > 0 && j >= 1 && j < 5) lds_st<f32x4>((q < 7 ? pc[q - 1] : pr[0]) + (j - 1) * 1024, acc[(q - 1) % 3][j - 1]);
                if constexpr ((nt & 1) && q >= 1 && q < 7 && j >= 7 && j < 9) acc[(q + 1) % 3][2 * j - 14 + (nt >> 1)] = lds_ld<f32x4>(pc[q + 1] + (2 * j - 14 + (nt >> 1)) * 1024);
                SB();
            });
            if constexpr (j == 6 && q < 7) BARRIER();
            if constexpr (i == 72) { yearly = __all((yfl.x == tA) & (yfl.y == tB)); if (yearly) y_load(); }
            SB();
        });
        STAMP(7);
        if (!yearly) { poll_flags(ws, yfo, tA, tB); y_load(); }
        SB();
        {
            f32x4 f0[4], f1[4];
#pragma unroll
            for (int nt = 0; nt < 4; ++nt) f0[nt] = lds_ld<f32x4>(pr[1] + nt * 1024);
#pragma unroll
            for (int nt = 0; nt < 4; ++nt) lds_st<f32x4>(pr[0] + nt * 1024, acc[7 % 3][nt]);
#pragma unroll
            for (int nt = 0; nt < 4; ++nt) f1[nt] = lds_ld<f32x4>(pr[1] + nt * 1024);
            SB();
#pragma unroll
            for (int nt = 0; nt < 4; ++nt) add_part(0, nt, f0[nt]);
#pragma unroll
            for (int nt = 0; nt < 4; ++nt) add_part(1, nt, f1[nt]);
        }
        SB();
#pragma unroll
        for (int i = 0; i < 4; ++i) lds_st<v4u>(ywa + i * 1024, yq[i]);
        STAMP(8);
        const rsrc_t ro = mk(p.out, SEQ * DM * 4);
#pragma unroll
        for (int e = 0; e < 2; ++e) {
            f32x4 oo[4];
#pragma unroll
            for (int i = 0; i < 8; ++i) {
                const unsigned yv = lds_ld<unsigned>((pr[2] ^ ((e ^ flip) * 64)) + (i >> 1) * 1024 + (i & 1) * 128);
                const f32x2 t = f32x2{o[e][2 * i], o[e][2 * i + 1]} + f32x2{bflo(yv), bfhi(yv)} * omg[e];
                oo[i >> 1][2 * (i & 1)] = t.x; oo[i >> 1][2 * (i & 1) + 1] = t.y;
            }
            PIN2(oo[0], oo[1]); PIN2(oo[2], oo[3]);
            SB();
#pragma unroll
            for (int nt = 0; nt < 4; ++nt)
                __builtin_amdgcn_raw_buffer_store_b128(__builtin_bit_cast(v4u, oo[nt]), ro, tok * 2560 + c4 * 16, ct * 256 + nt * 64 + (e ^ flip) * 16 * 2560, 2);
            asm volatile("s_nop 1");
            SB();
        }
        CODE_MARK(a);
    } else if (__builtin_expect(role < 30, 1)) {
        const int jj = r - 20, grp = xl * 10 + jj, H = xl >> 2, tb = 2 * (xl & 3) + H;
        const unsigned bw = w * BW_STRIDE;
        STAMP(10);
        const unsigned ldt2 = *(const __attribute__((address_space(4))) unsigned*)((uintptr_t)p.sldt + (grp * 8 + 2 * w) * 2);
        const rsrc_t rpf = mk(jj < 5 ? p.bre : p.bim, MBYTES);
        const unsigned pfo = (320 * H + 64 * (jj < 5 ? jj : jj - 5)) * 1280;
        const rsrc_t rc0 = mk((w & 1) ? p.cim : p.cre, MBYTES), rc1 = mk((w & 1) ? p.cre : p.cim, MBYTES);
        KgAddr ka;
        {
            const int j = lane & 31, hh = lane >> 5;
            const unsigned tab = bw + BW_TAB;
            ka.ra = bw + BW_STG + hh * STG_CH + j * 16;
            ka.t0 = tab + (8 * j + 4 * hh) * 2;
            ka.t1 = tab + (hh ? 384 + 2 * j : 256 + 4 * j) * 2; ka.t2 = tab + (hh ? 448 + 2 * j : 258 + 4 * j) * 2;
            ka.t3 = tab + (hh ? 640 + j : 512 + j) * 2;
            ka.tb = tab - 2 * 0x4280;
            ka.wa = bw + (lane & 3) * KEXP_RHO + (lane >> 2) * 16;
            const int lb = 2044 + (lane & 3) - 32 * (lane >> 2);
            f32x2 lf = {(float)lb, (float)(lb - 4)};
            PIN1(lf);
#pragma unroll
            for (int n = 0; n < 16; ++n) {
                const int o = 512 * (n >> 2) + 8 * (n & 3);
                unsigned pk = pk2v(lf - (float)o);
                if (n >= 12 && lb - o < 256) pk = (unsigned)(lb - o + 0x4280) | (unsigned)(lb - o - 4 + 0x4280) << 16;
                ka.lag[n] = pk;
                PIN1(ka.lag[n]);
            }
        }
        PIN2(ka.ra, ka.t0); PIN2(ka.t1, ka.t2); PIN2(ka.t3, ka.tb); PIN1(ka.wa);
        unsigned sa = bw + BW_STG + (lane >> 1) * STG_CH + (lane & 1) * 8, xta[4];
#pragma unroll
        for (int cs = 0; cs < 4; ++cs) {
            const int row = 8 * (lane >> 4) + ((lane >> 2) & 3);
            const unsigned f = (((row >> 1) & 1) | (((row >> 3) & 1) << 1)) << 1;
            xta[cs] = bw + row * 128 + (((2 * cs + ((lane & 3) >> 1)) ^ f) * 16) + (lane & 1) * 8;
            PIN1(xta[cs]);
        }
        unsigned xvo = ((lane >> 3) & 1) * 32768 + (lane & 7) * 16 + (lane >> 4) * 128;
        unsigned ua = ((lane & 7) >> 1) * BW_STRIDE + BW_U + (lane & 1) * U_CH + 64 + (64 * w + (lane >> 3)) * 16;
        PIN2(sa, xvo); PIN1(ua);
        WAITV(8);
        KgIn in[2];
#pragma unroll
        for (int e = 0; e < 2; ++e) {
            const unsigned a = bw + BW_PAR + e * 256 + lane * 4;
            in[e].are = lds_ld<unsigned>(a); in[e].aim = lds_ld<unsigned>(a + 1024); in[e].bre = lds_ld<unsigned>(a + 2048);
            in[e].bim = lds_ld<unsigned>(a + 3072); in[e].cre = lds_ld<unsigned>(a + 4096); in[e].cim = lds_ld<unsigned>(a + 5120);
            in[e].ldt = e ? ldt2 >> 16 : ldt2 & 0xffffu;
        }
        {
            const v2u ep = lds_ld<v2u>(bw + BW_PAR + 6144);
            tA = rfl(ep.x) + 1; tB = rfl(ep.y) + 1;
        }
        STAMP(1);
        LT(0);
        v4u xq[8];
        auto xt_store = [&](int i) {
            __builtin_amdgcn_raw_buffer_store_b128(xq[i], ws, xvo, WS_XT + (jj * 8 + 2 * (i & 3)) * 32768 + (tb * 8 + 2 * w + (i >> 2)) * 512, AUX_SC1);
            asm volatile("s_nop 1");
        };
        KgState ks = kg_lo(in[0], kg_head(in[0]), sa, [&](auto K) {
            if constexpr (K.v == 0) TOUCH(lane * 1280, rpf, pfo + (w + 3) * 128);
            if constexpr (K.v == 2) TOUCH(w < 3 ? lane * 1280 : 0x40000000, rpf, pfo + (w + 7) * 128);
            if constexpr (K.v == 3) {
                WAITV(2);
#pragma unroll
                for (int i = 0; i < 8; ++i) {
                    const v2u x0 = lds_tr(xta[i & 3] + (i >> 2) * 4096), x1 = lds_tr(xta[i & 3] + (i >> 2) * 4096 + 512);
                    xq[i] = v4u{x0.x, x0.y, x1.x, x1.y};
                }
            }
            if constexpr (K.v >= 4) { xt_store(2 * K.v - 8); xt_store(2 * K.v - 7); }
            if constexpr (K.v == 0 || K.v == 2 || K.v == 3 || K.v == 7) LT(K.v == 0 ? 1 : K.v == 7 ? 4 : K.v);
        });
        STAMP(2);
        LT(5);
        kg_hi(ks, sa, [&](auto R) {
            if constexpr (R.v == XFR) {
                LT(6);
                asm volatile("s_waitcnt vmcnt(0)\n\ts_barrier" ::: "memory");
                set_flag(ws, FL_XT + tb * 10 + jj, tA, tB);
                LT(7);
                STAMP(7);
            }
            if constexpr (R.v == XFR + 2) touch(lane < 40 ? (lane >> 2) * 128 + (lane & 3) * 1280 : 0x40000000, rc0, (grp * 8 + (w >> 1) * 4) * 1280);
        });
        auto c_l2 = [&](int pb) {
        };
        LT(8);
        c_l2(0);
        KgHead hd; float ht[8]; unsigned tp[32];
        unsigned xfo = WS_FLAG + (FL_XT + (lane & 7) * 10 + (grp >> 3)) * 256;
        v2u xfl;
        kg_product(ka, tp, [&](auto KS) {
            if constexpr (KS.v >= 1 && KS.v < 12) kg_head_step<KS.v - 1>(in[1], hd, ht);
        });
        STAMP(3);
        LT(9);
        v4u uu[8];
        ks = kg_lo(in[1], hd, sa, [&](auto K) {
            if constexpr (K.v == 0) {
#pragma unroll
                for (int r = 0; r < 4; ++r) kg_expand(ka, BW_K1, tp, r);
                if (lane < 28) lds_st<v4u>(ka.wa + BW_K1 + 1024, v4u{0, 0, 0, 0});
            }
            if constexpr (K.v == XPK - 2) xfl = __builtin_amdgcn_raw_buffer_load_b64(ws, xfo, 0, AUX_SC1);
            if constexpr (K.v == XPK) {
                if (!__all((xfl.x == tA) & (xfl.y == tB))) poll_flags(ws, xfo, tA, tB);
                __builtin_amdgcn_fence(__ATOMIC_ACQUIRE, "wavefront");
                MEMFENCE();
            }
            if constexpr (K.v >= XPK && K.v < XPK + 2) {
#pragma unroll
                for (int i = 4 * (K.v - XPK); i < 4 * (K.v - XPK) + 4; ++i)
                    uu[i] = __builtin_amdgcn_raw_buffer_load_b128(ws, lane * 16, WS_XT + grp * 32768 + w * 8192 + i * 1024, AUX_SC1);
            }
            if constexpr (K.v >= XPK + 2 && K.v < XPK + 4) {
#pragma unroll
                for (int i = 4 * (K.v - XPK - 2); i < 4 * (K.v - XPK - 2) + 4; ++i) lds_st<v4u>(ua + i * 128, uu[i]);
            }
            LT(10 + K.v);
        });
        static_assert(XPK + 4 <= 8, "");
        PIN2(ks.w.r, ks.w.i); PIN2(ks.e1.r, ks.e1.i); PIN2(ks.e2.r, ks.e2.i); PIN2(ks.e4.r, ks.e4.i); PIN2(ks.e256.r, ks.e256.i);
        if (lane < 8) lds_st<v4u>(bw + BW_U + (lane >> 2) * U_CH + (lane & 3) * 16, v4u{0, 0, 0, 0});
        BARRIER();
        STAMP(4);
        LT(18);
        MEMFENCE();
        Conv cv1, cv2;
        cv1.init(bw + BW_K1, bw + BW_U, lane);
        cv2.init(bw + BW_K2, bw + BW_U + U_CH, lane);
        f32x16 acc1[8];
        {
            float hr[21][2], hi[21][2];
            hr[0][0] = ks.w.r.x; hr[0][1] = ks.w.r.y; hi[0][0] = ks.w.i.x; hi[0][1] = ks.w.i.y;
            f32x16 d;
            v4u ta[2], tb2[2];
            unsigned t[32];
            conv_first(cv1, acc1, [&](auto M, auto FI) {
                constexpr int m = M.v, f = FI.v;
                if constexpr (f >= 0 && f < 52) {
                    constexpr int pr = f / 5, q = f % 5;
                    if constexpr (q < 4) {
                        constexpr int r = 2 * pr + 1 + (q >> 1), md = q & 1;
                        if constexpr (r < 21) {
                            const cp2& mu = hmul(ks, HMUL[r]);
                            const float br = md ? mu.r.y : mu.r.x, bi = md ? mu.i.y : mu.i.x;
                            const cpx o = cmulv(cpx{hr[HSRC[r]][md], hi[HSRC[r]][md]}, cpx{br, bi});
                            hr[r][md] = o.r; hi[r][md] = o.i;
                        }
                    } else {
                        lds_st<v2u>(sa + 2 * pr * 16, v2u{pk2(hr[2 * pr][0], hr[2 * pr][1]), pk2(hi[2 * pr][0], hi[2 * pr][1])});
                        lds_st<v2u>(sa + (2 * pr + 1) * 16, v2u{pk2(hr[2 * pr + 1][0], hr[2 * pr + 1][1]), pk2(hi[2 * pr + 1][0], hi[2 * pr + 1][1])});
                    }
                    if constexpr (f == 50) lds_st<v2u>(sa + 20 * 16, v2u{pk2(hr[20][0], hr[20][1]), pk2(hi[20][0], hi[20][1])});
                    if constexpr (f == 50 || f == 51) { ta[f & 1] = lds_ld<v4u>(ka.ra + (f - 50) * 2 * STG_CH); tb2[f & 1] = lds_ld<v4u>(ka.ra + (f - 50) * 2 * STG_CH + 21 * 16); }
                } else if constexpr (f >= 52 && f < 68) {
                    constexpr int k = f - 52;
                    d = mfma32<k == 0>(ta[k & 1], tb2[k & 1], d);
                    SB();
                    if constexpr (k < 14) { ta[k & 1] = lds_ld<v4u>(ka.ra + (k + 2) * 2 * STG_CH); tb2[k & 1] = lds_ld<v4u>(ka.ra + (k + 2) * 2 * STG_CH + 21 * 16); }
                } else if constexpr (f == 69) {
                    asm volatile("s_nop 7" : "+v"(d));
                } else if constexpr (f >= 70 && f < 74) {
                    kg_scatter(ka, d, f - 70);
                } else if constexpr (f >= 74 && f < 92) {
                    constexpr int n = 2 * (f - 74);
                    if constexpr (n < 32) { t[n] = lds_ld<u16>(tab_addr(ka, n)); t[n + 1] = lds_ld<u16>(tab_addr(ka, n + 1)); }
                    if constexpr (n >= 4) { t[n - 4] |= t[n - 3] << 16; PIN1(t[n - 4]); }
                    if constexpr (n >= 10 && (n - 10) % 8 == 0) {
                        constexpr int r = (n - 10) / 8;
                        lds_st<v4u>(ka.wa + BW_K2 + r * 256, v4u{t[8 * r], t[8 * r + 2], t[8 * r + 4], t[8 * r + 6]});
                    }
                } else if constexpr (f == 68) {
                    if (lane < 28) lds_st<v4u>(ka.wa + BW_K2 + 1024, v4u{0, 0, 0, 0});
                }
                if constexpr (m == 7) c_l2(2);
                if constexpr (m == 43) c_l2(3);
                if constexpr (m == 79) c_l2(4);
            });
        }
        STAMP(12);
        LT(19);
        {
            f32x16 acc[8];
            v4u af[2][8], bq[8], yq[2];
            unsigned ysa = bw + BW_Y + lane * 4, yra = bw + BW_Y + lane * 16;
            PIN2(ysa, yra);
            const unsigned yso = WS_Y + (grp * 4 + w) * 8192;
            auto op = [&](auto O) {
                constexpr int o = O.v;
                if constexpr (o < 48) cv1.template epi<48 + o>(acc1[4 + o / 12], acc1[3 + o / 12][15]);
                else {
                    constexpr int tl = (o - 48) / 15, kind = (o - 48) % 15;
                    if constexpr (kind == 0 && tl == 4) asm volatile("s_nop 4" : "+v"(acc[4]));
                    if constexpr (kind < 12) cv2.template epi<12 * tl + kind>(acc[tl], acc[tl ? tl - 1 : 0][15]);
                    else if constexpr (kind == 12) {
#pragma unroll
                        for (int G = 4 * tl; G < 4 * tl + 4; ++G) lds_st<unsigned>(ysa + G * 256, pk2(cv1.y[G], cv2.y[G]));
                    } else if constexpr (kind == 13) yq[tl & 1] = lds_ld<v4u>(yra + tl * 1024);
                    else { __builtin_amdgcn_raw_buffer_store_b128(yq[tl & 1], ws, lane * 16, yso + tl * 1024, AUX_SC1); asm volatile("s_nop 1"); }
                }
            };
#pragma unroll
            for (int dl = 0; dl < 4; ++dl) af[0][dl] = cv2.ldA(0, dl);
            bq[0] = cv2.ldB(0, 0); bq[1] = cv2.ldB(0, 1);
            SB();
            sfor<0, 144>([&](auto M) {
                constexpr int m = M.v, c = S2T.c[m], t = S2T.t[m], i = S2T.i[m];
                constexpr bool lo = m < 40, last = m >= 118;
                constexpr int gi = lo ? 4 * c + i : 16 + 8 * c + i, bs = last ? (i + 3) & 7 : gi % 3;
                acc[t] = mfma32<c == 0 && i == 0>(af[c & 1][t - i], bq[bs], acc[t]);
                SB();
                if constexpr (S2T.rd[m]) {
                    constexpr int g3 = gi + 3;
                    if constexpr (g3 < 16) bq[gi % 3] = cv2.ldB(g3 >> 2, g3 & 3);
                    else if constexpr (g3 < 40) bq[gi % 3] = cv2.ldB((g3 - 16) >> 3, (g3 - 16) & 7);
                    if constexpr (gi >= 34 && gi < 39) bq[gi - 31] = cv2.ldB(3, gi - 34);
                    if constexpr (!lo) af[(c + 1) & 1][7 - i] = cv2.ldA(c + 1, 7 - i);
                    else if constexpr (c < 3) af[(c + 1) & 1][i] = cv2.ldA(c + 1, i);
                    else af[0][4 + i] = cv2.ldA(0, 4 + i);
                }
                if constexpr (m == 0) bq[2] = cv2.ldB(0, 2);
                if constexpr (m >= 40 && m < 43) af[0][43 - m] = cv2.ldA(0, 43 - m);
                if constexpr (m == 44) af[0][0] = cv2.ldA(0, 0);
                if constexpr (m >= 118 && m < 121) bq[m - 118] = cv2.ldB(3, m - 113);
                sfor<S2T.start[m], S2T.start[m + 1]>([&](auto K) { op(IC<S2T.order[K.v]>{}); });
                SB();
            });
            asm volatile("s_nop 7" : "+v"(acc[7]));
            sfor<S2T.start[144], S2T.start[145]>([&](auto K) { op(IC<S2T.order[K.v]>{}); });
        }
        STAMP(5);
        asm volatile("s_waitcnt vmcnt(0) lgkmcnt(0)\n\ts_barrier" ::: "memory");
        set_flag(ws, FL_Y + grp, tA, tB);
        STAMP(6);
        CODE_MARK(b);
    } else {
        const int gc = 2 * (2 * (xl & 3) + 1 - (xl >> 2)) + (r - 30);
        const v2u ep = __builtin_amdgcn_raw_buffer_load_b64(ws, WS_EPOCH, 0, AUX_SC1);
        const rsrc_t rgw = mk(p.gw, VBYTES);
        const unsigned vo = (lane >> 3) * 1280 + (lane & 7) * 16, so = (128 * gc + 32 * w) * 1280;
        float sum[4] = {0.f, 0.f, 0.f, 0.f};
        v4u gv[10], xv[10][4];
#pragma unroll
        for (int s = 0; s < 10; ++s) {
            gv[s] = __builtin_amdgcn_raw_buffer_load_b128(rgw, (lane & 7) * 16, ((s + 3) % 10) * 128, 0);
#pragma unroll
            for (int q = 0; q < 4; ++q) xv[s][q] = __builtin_amdgcn_raw_buffer_load_b128(rx, vo, so + q * 8 * 1280 + ((s + 3) % 10) * 128, PFNT ? 2 : 0);
        }
        SB();
#pragma unroll
        for (int s = 0; s < 10; ++s)
#pragma unroll
            for (int e = 0; e < 4; ++e)
#pragma unroll
                for (int q = 0; q < 4; ++q) {
                    const unsigned xa = xv[s][q][e], ga = gv[s][e];
                    sum[q] = __builtin_amdgcn_fdot2_f32_bf16(__builtin_bit_cast(bf16x2, xa), __builtin_bit_cast(bf16x2, ga), sum[q], false);
                }
        tA = rfl(ep.x) + 1; tB = rfl(ep.y) + 1;
#pragma unroll
        for (int q = 0; q < 4; ++q) {
            float v = sum[q];
            v += __shfl_xor(v, 1); v += __shfl_xor(v, 2); v += __shfl_xor(v, 4);
            const float z = rbf(v), gt = rbf(__builtin_amdgcn_rcpf(1.f + __expf(-z)));
            if ((lane & 7) == 0)
                __builtin_amdgcn_raw_buffer_store_b32(__float_as_uint(gt), ws, WS_G + (128 * gc + 32 * w + 8 * q + (lane >> 3)) * 4, 0, AUX_SC1);
        }
        asm volatile("s_waitcnt vmcnt(0) lgkmcnt(0)\n\ts_barrier" ::: "memory");
        set_flag(ws, FL_G + gc, tA, tB);
        STAMP(5);
        if (b == 255 && w == 0) {
#pragma unroll 1
            for (int k = 3; k >= 0; --k) {
                const int i = k * 64 + lane;
                const unsigned off = WS_FLAG + (i < 240 ? i : i + 80) * 256;
#pragma unroll 1
                for (int spin = 0; spin < SPIN_CAP; ++spin) {
                    MEMFENCE();
                    const v2u f = __builtin_amdgcn_raw_buffer_load_b64(ws, off, 0, AUX_SC1);
                    if (__all((f.x == tA) & (f.y == tB))) break;
                    __builtin_amdgcn_s_sleep(16);
                }
            }
            if (lane == 0) __builtin_amdgcn_raw_buffer_store_b64(v2u{tA, tB}, ws, WS_EPOCH, 0, AUX_SC1);
        }
        CODE_MARK(g);
    }
}

extern "C" void kernel_launch(void* const* d_in, const int* in_sizes, int n_in, void* d_out, int out_size, void* d_ws,
                              size_t ws_size, hipStream_t stream) {
    Params p;
    p.x = d_in[0]; p.lre = d_in[1]; p.lim = d_in[2]; p.bre = d_in[3]; p.bim = d_in[4]; p.cre = d_in[5]; p.cim = d_in[6];
    p.ldt = d_in[7]; p.dska = d_in[8];
    p.sare = d_in[10]; p.saim = d_in[11]; p.sbre = d_in[12]; p.sbim = d_in[13]; p.scre = d_in[14]; p.scim = d_in[15];
    p.sldt = d_in[16]; p.dskb = d_in[17]; p.gw = d_in[18];
    p.out = d_out; p.ws = d_ws;
    (void)hipFuncSetAttribute((const void*)k_fused, hipFuncAttributeMaxDynamicSharedMemorySize, LDS_BYTES);
    k_fused<<<256, 256, LDS_BYTES, stream>>>(p);
}
```
